```python
import torch

def _softmax(x, axis=-1):
    return torch.softmax(x, dim=axis)
def _gelu(x):
    return torch.nn.functional.gelu(x, approximate="tanh")
def _silu(x):
    return torch.nn.functional.silu(x)
def _sigmoid(x):
    return torch.sigmoid(x)
def _layernorm(x, axis=-1, eps=1e-5):
    m = x.mean(dim=axis, keepdim=True)
    v = x.var(dim=axis, keepdim=True, unbiased=False)
    return (x - m) / torch.sqrt(v + eps)
def _scatter_add(tgt, idx, src, axis=0):
    return tgt.clone().index_add_(axis, idx, src)
def _segment_sum(src, seg, n):
    out = torch.zeros((n,) + src.shape[1:], dtype=src.dtype)
    return out.index_add_(0, seg, src)
def _pad(x, pw):
    flat = tuple(v for pair in reversed(pw) for v in pair)
    return torch.nn.functional.pad(x, flat)

kernel_name = 'sequential_preLN__mha_block_cached__dense_activation_silu__decode'

def setup_inputs():

    _w = (lambda a: (lambda t: t.to(torch.bfloat16) if t.is_floating_point() else t)(torch.as_tensor(a)))
    def _A_setup():
        B, Sq, D = 4, 1, 448
        H, Sk, Dh = 16, 8192, 256
        return {
            "x": _w((lambda *s: torch.randn(*s, dtype=torch.bfloat16) * 0.05)(B, Sq, D)),
            "k_cache": _w((lambda *s: torch.randn(*s, dtype=torch.bfloat16) * 3.2)(B, H, Sk, Dh)),
            "v_cache": _w((lambda *s: torch.randn(*s, dtype=torch.bfloat16) * 0.8)(B, H, Sk, Dh)),
            "wq": _w((lambda *s: torch.randn(*s, dtype=torch.bfloat16) * 3.2)(D, H * Dh) * (D ** -0.5)),
            "wo": _w((lambda *s: torch.randn(*s, dtype=torch.bfloat16) * 0.8)(H * Dh, D) * ((H * Dh) ** -0.5)),
        }
    _A_in = _A_setup()

    def _B_setup():
        B, S, D, F = 4, 1, 448, 1792
        return {
            "x": _w((lambda *s: torch.randn(*s, dtype=torch.bfloat16) * 0.05)(B, S, D)),
            "w1": _w((lambda *s: torch.randn(*s, dtype=torch.bfloat16) * 3.2)(D, F) * (D ** -0.5)),
            "w2": _w((lambda *s: torch.randn(*s, dtype=torch.bfloat16) * 0.8)(F, D) * (F ** -0.5)),
        }
    _B_in = _B_setup()

    return {
        "A_x": _A_in["x"],
        "A_k_cache": _A_in["k_cache"],
        "A_v_cache": _A_in["v_cache"],
        "A_wq": _A_in["wq"],
        "A_wo": _A_in["wo"],
        "B_x": _B_in["x"],
        "B_w1": _B_in["w1"],
        "B_w2": _B_in["w2"],
    }

def reference(A_x, A_k_cache, A_v_cache, A_wq, A_wo, B_x, B_w1, B_w2):
    def _A_ref(x, k_cache, v_cache, wq, wo):
        B, Sq, D = x.shape
        H, Dh = 16, 256
        q = (lambda x, *d: x.permute(*d))((x @ wq).reshape(B, Sq, H, Dh), 0, 2, 1, 3)
        scores = torch.einsum("bhqd,bhkd->bhqk", q, k_cache) * (Dh ** -0.5)
        p = _softmax(scores, axis=-1)
        o = torch.einsum("bhqk,bhkd->bhqd", p, v_cache)
        return (lambda x, *d: x.permute(*d))(o, 0, 2, 1, 3).reshape(B, Sq, H * Dh) @ wo

    def _B_ref(x, w1, w2):
        h = torch.einsum("bsd,df->bsf", x, w1)
        h = _silu(h)
        out = torch.einsum("bsf,fd->bsd", h, w2)
        return out

    x = A_x
    h = x + _A_ref(_layernorm(x, axis=-1), A_k_cache, A_v_cache, A_wq, A_wo)
    h2 = h + _B_ref(_layernorm(h, axis=-1), B_w1, B_w2)
    return h2
```

```cpp
#include <hip/hip_runtime.h>
#include <hip/hip_bf16.h>
#include <stdint.h>

typedef unsigned u32x4 __attribute__((ext_vector_type(4)));
typedef float f32x4 __attribute__((ext_vector_type(4)));
typedef __bf16 bf16x2 __attribute__((ext_vector_type(2)));
typedef unsigned short u16;

constexpr int NB = 4, DM = 448, NH = 16, SK = 8192, DH = 256, FF = 1792, HD = NH * DH;
constexpr int NSPLIT = 4, KPW = SK / NSPLIT;
constexpr int NWAVE = 8, TILE = 32, NT = KPW / (TILE * NWAVE);

__device__ __forceinline__ float bf2f(unsigned b) { return __builtin_bit_cast(float, b << 16); }
__device__ __forceinline__ float bflo(unsigned w) { return __builtin_bit_cast(float, w << 16); }
__device__ __forceinline__ float bfhi(unsigned w) { return __builtin_bit_cast(float, w & 0xffff0000u); }
__device__ __forceinline__ u16 f2bf(float f) { __bf16 b = (__bf16)f; return __builtin_bit_cast(u16, b); }
__device__ __forceinline__ float rbf(float f) { return bf2f(f2bf(f)); }
__device__ __forceinline__ float dot2(unsigned a, unsigned b, float c) {
  return __builtin_amdgcn_fdot2_f32_bf16(__builtin_bit_cast(bf16x2, a), __builtin_bit_cast(bf16x2, b), c, false);
}
template <int CTRL, int BANK> __device__ __forceinline__ float dppu(float old, float x) {
  return __builtin_bit_cast(float, __builtin_amdgcn_update_dpp(__builtin_bit_cast(int, old), __builtin_bit_cast(int, x), CTRL, 0xf, BANK, false));
}
template <int CTRL> __device__ __forceinline__ float dpp(float x) {
  return __builtin_bit_cast(float, __builtin_amdgcn_mov_dpp(__builtin_bit_cast(int, x), CTRL, 0xf, 0xf, true));
}
constexpr int XOR1 = 0xB1, XOR2 = 0x4E, XOR7 = 0x141, ROR8 = 0x128, ROR4 = 0x124, ROR12 = 0x12C;
__device__ __forceinline__ float wave_sum(float x) {
  x += dpp<XOR1>(x); x += dpp<XOR2>(x); x += dpp<XOR7>(x); x += dpp<ROR8>(x);
  auto s = __builtin_amdgcn_permlane16_swap(__float_as_uint(x), __float_as_uint(x), false, false);
  x = __uint_as_float(s[0]) + __uint_as_float(s[1]);
  auto t = __builtin_amdgcn_permlane32_swap(__float_as_uint(x), __float_as_uint(x), false, false);
  return __uint_as_float(t[0]) + __uint_as_float(t[1]);
}

__device__ __forceinline__ void layernorm7(const float (&x)[7], float (&o)[7]) {
  float s = 0.f;
#pragma unroll
  for (int j = 0; j < 7; ++j) s += x[j];
  const float mean = wave_sum(s) * (1.0f / DM);
  float v = 0.f;
#pragma unroll
  for (int j = 0; j < 7; ++j) { const float d = x[j] - mean; v += d * d; }
  const float var = wave_sum(v) * (1.0f / DM);
  const float mb = rbf(mean), vb = rbf(var);
  const float sd = rbf(sqrtf(rbf(vb + 1e-5f)));
#pragma unroll
  for (int j = 0; j < 7; ++j) o[j] = rbf(rbf(x[j] - mb) / sd);
}

template <int K, int N>
__device__ __forceinline__ void gemv64(const float* a_lds, const u16* W, int n0, float* red  , float (&res)[1], int tid) {
  const int cg = tid & 7, rg = tid >> 3;
  float acc[4][8];
#pragma unroll
  for (int b = 0; b < 4; ++b)
#pragma unroll
    for (int j = 0; j < 8; ++j) acc[b][j] = 0.f;
#pragma unroll 7
  for (int k = rg; k < K; k += 32) {
    const u32x4 w = *reinterpret_cast<const u32x4*>(W + (size_t)k * N + n0 + cg * 8);
#pragma unroll
    for (int b = 0; b < 4; ++b) {
      const float av = a_lds[b * K + k];
#pragma unroll
      for (int j = 0; j < 4; ++j) { acc[b][2 * j] = fmaf(av, bflo(w[j]), acc[b][2 * j]); acc[b][2 * j + 1] = fmaf(av, bfhi(w[j]), acc[b][2 * j + 1]); }
    }
  }
#pragma unroll
  for (int b = 0; b < 4; ++b)
#pragma unroll
    for (int j = 0; j < 8; ++j) red[(rg * 4 + b) * 64 + cg * 8 + j] = acc[b][j];
  __syncthreads();
  float s = 0.f;
  for (int r = 0; r < 32; ++r) s += red[r * 256 + tid];
  res[0] = s;
}

__global__ void __launch_bounds__(256) k_q(const u16* x, const u16* wq, u16* q) {
  __shared__ float a[4 * DM];
  __shared__ float red[32 * 256];
  const int tid = threadIdx.x, lane = tid & 63, w = tid >> 6;
  float xv[7], o[7];
#pragma unroll
  for (int j = 0; j < 7; ++j) xv[j] = bf2f(x[w * DM + lane + 64 * j]);
  layernorm7(xv, o);
#pragma unroll
  for (int j = 0; j < 7; ++j) a[w * DM + lane + 64 * j] = o[j];
  __syncthreads();
  float r[1];
  gemv64<DM, HD>(a, wq, blockIdx.x * 64, red, r, tid);
  q[w * HD + blockIdx.x * 64 + lane] = f2bf(r[0]);
}


#define LDB(rs, voff, soff) __builtin_amdgcn_raw_buffer_load_b128(rs, voff, soff, 2)
__global__ void __launch_bounds__(512, 2) k_attn(const u16* __restrict__ q, const u16* __restrict__ kc, const u16* __restrict__ vc, float* __restrict__ part) {
  __shared__ float mrg[16 * (DH + 4)];
  const int tid = threadIdx.x, lane = tid & 63, w = __builtin_amdgcn_readfirstlane(tid >> 6);
  const int bh = blockIdx.x >> 2, sp = blockIdx.x & 3;
  const u32x4 qv = *reinterpret_cast<const u32x4*>(q + bh * DH + (lane & 31) * 8);
  const size_t base = ((size_t)bh * SK + (size_t)sp * KPW) * DH;
  const __amdgpu_buffer_rsrc_t rk = __builtin_amdgcn_make_buffer_rsrc((void*)(kc + base), 0, KPW * DH * 2, 0x00020000);
  const __amdgpu_buffer_rsrc_t rv = __builtin_amdgcn_make_buffer_rsrc((void*)(vc + base), 0, KPW * DH * 2, 0x00020000);
  const int voff = lane * 16;
  int soff = w * (TILE * DH * 2);
  constexpr int TSTR = TILE * NWAVE * DH * 2;
  u32x4 K[16], V[16];
#pragma unroll
  for (int i = 0; i < 16; ++i) K[i] = LDB(rk, voff + (i & 3) * 1024, soff + (i >> 2) * 4096);
#pragma unroll
  for (int i = 0; i < 16; ++i) V[i] = LDB(rv, voff + (i & 3) * 1024, soff + (i >> 2) * 4096);
  asm volatile("" ::: "memory");
  __builtin_amdgcn_sched_barrier(0);
  float m = -INFINITY, l = 0.f, acc[8];
#pragma unroll
  for (int j = 0; j < 8; ++j) acc[j] = 0.f;
  const int c = lane & 15;
  constexpr float L2E = 1.4426950408889634f;

  auto body = [&](bool more) {
    soff += TSTR;
    float x[16];
#pragma unroll
    for (int i = 0; i < 16; ++i) {
      float d = dot2(K[i][0], qv[0], 0.f);
      d = dot2(K[i][1], qv[1], d); d = dot2(K[i][2], qv[2], d); d = dot2(K[i][3], qv[3], d);
      x[i] = d;
      asm volatile("" : "+v"(x[i]) :: "memory");
      if (more) K[i] = LDB(rk, voff + (i & 3) * 1024, soff + (i >> 2) * 4096);
      __builtin_amdgcn_sched_barrier(0);
    }
#pragma unroll
    for (int h = 0; h < 8; ++h) x[h] = dppu<ROR8, 0xc>(x[h], x[h + 8]) + dppu<ROR8, 0x3>(x[h + 8], x[h]);
#pragma unroll
    for (int h = 0; h < 4; ++h) x[h] = dppu<XOR7, 0xa>(x[h], x[h + 4]) + dppu<XOR7, 0x5>(x[h + 4], x[h]);
#pragma unroll
    for (int h = 0; h < 2; ++h) {
      const float lo = x[h], hi = x[h + 2];
      const float t1 = lo + dpp<XOR2>(lo), t2 = hi + dpp<XOR2>(hi);
      x[h] = (c & 2) ? t2 : t1;
    }
    {
      const float lo = x[0], hi = x[1];
      const float t1 = lo + dpp<XOR1>(lo), t2 = hi + dpp<XOR1>(hi);
      x[0] = (c & 1) ? t2 : t1;
    }
    float sc;
    {
      auto s = __builtin_amdgcn_permlane16_swap(__float_as_uint(x[0]), __float_as_uint(x[0]), false, false);
      sc = __uint_as_float(s[0]) + __uint_as_float(s[1]);
    }
    sc = rbf(sc) * 0.0625f;
    float pm = sc;
    pm = fmaxf(pm, dpp<XOR1>(pm)); pm = fmaxf(pm, dpp<XOR2>(pm)); pm = fmaxf(pm, dpp<XOR7>(pm)); pm = fmaxf(pm, dpp<ROR8>(pm));
    const float mn = fmaxf(m, pm);
    const float alpha = __builtin_amdgcn_exp2f((m - mn) * L2E);
    const float p = __builtin_amdgcn_exp2f((sc - mn) * L2E);
    l = fmaf(l, alpha, p); m = mn;
#pragma unroll
    for (int j = 0; j < 8; ++j) acc[j] *= alpha;
#define PV(i) { const float pi = dpp<0x150 + i>(p); \
      _Pragma("unroll") for (int j = 0; j < 4; ++j) { acc[2 * j] = fmaf(pi, bflo(V[i][j]), acc[2 * j]); acc[2 * j + 1] = fmaf(pi, bfhi(V[i][j]), acc[2 * j + 1]); } \
      asm volatile("" : "+v"(acc[0]), "+v"(acc[1]), "+v"(acc[2]), "+v"(acc[3]), "+v"(acc[4]), "+v"(acc[5]), "+v"(acc[6]), "+v"(acc[7]) :: "memory"); \
      if (more) V[i] = LDB(rv, voff + (i & 3) * 1024, soff + (i >> 2) * 4096); __builtin_amdgcn_sched_barrier(0); }
    PV(0) PV(1) PV(2) PV(3) PV(4) PV(5) PV(6) PV(7) PV(8) PV(9) PV(10) PV(11) PV(12) PV(13) PV(14) PV(15)
#undef PV
  };
  for (int t = 0; t < NT - 1; ++t) body(true);
  body(false);
  l += dpp<XOR1>(l); l += dpp<XOR2>(l); l += dpp<XOR7>(l); l += dpp<ROR8>(l);
  float* rec = mrg + (w * 2 + (lane >> 5)) * (DH + 4);
  *reinterpret_cast<f32x4*>(rec + 4 + (lane & 31) * 8) = f32x4{acc[0], acc[1], acc[2], acc[3]};
  *reinterpret_cast<f32x4*>(rec + 4 + (lane & 31) * 8 + 4) = f32x4{acc[4], acc[5], acc[6], acc[7]};
  if ((lane & 31) == 0) { rec[0] = m; rec[1] = l; }
  __syncthreads();
  if (tid < DH) {
    float M = -INFINITY;
#pragma unroll
    for (int r = 0; r < 16; ++r) M = fmaxf(M, mrg[r * (DH + 4)]);
    float L = 0.f, o = 0.f;
#pragma unroll
    for (int r = 0; r < 16; ++r) { const float wt = __builtin_amdgcn_exp2f((mrg[r * (DH + 4)] - M) * L2E); L = fmaf(wt, mrg[r * (DH + 4) + 1], L); o = fmaf(wt, mrg[r * (DH + 4) + 4 + tid], o); }
    float* pr = part + (size_t)blockIdx.x * (DH + 4);
    pr[4 + tid] = o;
    if (tid == 0) { pr[0] = M; pr[1] = L; }
  }
}

__global__ void __launch_bounds__(256) k_wo(const float* part, const u16* wo, float* ypart) {
  __shared__ float a[4 * DH];
  __shared__ float red[32 * 256];
  const int tid = threadIdx.x, h = blockIdx.y;
  constexpr float L2E = 1.4426950408889634f;
  for (int b = 0; b < 4; ++b) {
    const float* pr = part + (size_t)((b * NH + h) * NSPLIT) * (DH + 4);
    float M = -INFINITY;
#pragma unroll
    for (int s = 0; s < NSPLIT; ++s) M = fmaxf(M, pr[s * (DH + 4)]);
    float L = 0.f, o = 0.f;
#pragma unroll
    for (int s = 0; s < NSPLIT; ++s) { const float wt = __builtin_amdgcn_exp2f((pr[s * (DH + 4)] - M) * L2E); L = fmaf(wt, pr[s * (DH + 4) + 1], L); o = fmaf(wt, pr[s * (DH + 4) + 4 + tid], o); }
    a[b * DH + tid] = rbf(o / L);
  }
  __syncthreads();
  float r[1];
  gemv64<DH, DM>(a, wo + (size_t)h * DH * DM, blockIdx.x * 64, red, r, tid);
  ypart[(h * 4 + (tid >> 6)) * DM + blockIdx.x * 64 + (tid & 63)] = r[0];
}

__global__ void __launch_bounds__(256) k_w1(const u16* x, const float* ypart, const u16* w1, float* hbuf, float* h1) {
  __shared__ float a[4 * DM];
  __shared__ float red[32 * 256];
  const int tid = threadIdx.x, lane = tid & 63, w = tid >> 6;
  float hv[7], o[7];
#pragma unroll
  for (int j = 0; j < 7; ++j) {
    float s = 0.f;
    for (int h = 0; h < NH; ++h) s += ypart[(h * 4 + w) * DM + lane + 64 * j];
    hv[j] = rbf(bf2f(x[w * DM + lane + 64 * j]) + rbf(s));
    if (blockIdx.x == 0) hbuf[w * DM + lane + 64 * j] = hv[j];
  }
  layernorm7(hv, o);
#pragma unroll
  for (int j = 0; j < 7; ++j) a[w * DM + lane + 64 * j] = o[j];
  __syncthreads();
  float r[1];
  gemv64<DM, FF>(a, w1, blockIdx.x * 64, red, r, tid);
  const float t = rbf(r[0]);
  h1[w * FF + blockIdx.x * 64 + lane] = rbf(t / (1.0f + __expf(-t)));
}

__global__ void __launch_bounds__(256) k_w2(const float* hbuf, const float* h1, const u16* w2, u16* out) {
  __shared__ float a[4 * FF];
  __shared__ float red[32 * 256];
  const int tid = threadIdx.x, lane = tid & 63, w = tid >> 6;
  for (int i = tid; i < 4 * FF; i += 256) a[i] = h1[i];
  __syncthreads();
  float r[1];
  gemv64<FF, DM>(a, w2, blockIdx.x * 64, red, r, tid);
  const int col = blockIdx.x * 64 + lane;
  out[w * DM + col] = f2bf(hbuf[w * DM + col] + rbf(r[0]));
}

extern "C" void kernel_launch(void* const* d_in, const int* in_sizes, int n_in, void* d_out, int out_size, void* d_ws, size_t ws_size, hipStream_t stream) {
  const u16* x = (const u16*)d_in[0];
  const u16* kc = (const u16*)d_in[1];
  const u16* vc = (const u16*)d_in[2];
  const u16* wq = (const u16*)d_in[3];
  const u16* wo = (const u16*)d_in[4];
  const u16* w1 = (const u16*)d_in[6];
  const u16* w2 = (const u16*)d_in[7];
  char* ws = (char*)d_ws;
  u16* q = (u16*)ws;
  float* part = (float*)(ws + 65536);
  float* ypart = (float*)(ws + 65536 + 524288);
  float* hbuf = (float*)(ws + 65536 + 524288 + 131072);
  float* h1 = hbuf + 4 * DM;
  k_q<<<HD / 64, 256, 0, stream>>>(x, wq, q);
  k_attn<<<NB * NH * NSPLIT, 512, 0, stream>>>(q, kc, vc, part);
  k_wo<<<dim3(DM / 64, NH), 256, 0, stream>>>(part, wo, ypart);
  k_w1<<<FF / 64, 256, 0, stream>>>(x, ypart, w1, hbuf, h1);
  k_w2<<<DM / 64, 256, 0, stream>>>(hbuf, h1, w2, (u16*)d_out);
}
```

```cpp
#include <hip/hip_runtime.h>
#include <hip/hip_bf16.h>
#include <stdint.h>

typedef unsigned u32x4 __attribute__((ext_vector_type(4)));
typedef unsigned u32x2 __attribute__((ext_vector_type(2)));
typedef float f32x4 __attribute__((ext_vector_type(4)));
typedef __bf16 bf16x2 __attribute__((ext_vector_type(2)));
typedef unsigned short u16;
typedef __amdgpu_buffer_rsrc_t rsrc_t;

constexpr int NB = 4, DM = 448, NH = 16, SK = 8192, DH = 256, FF = 1792, HD = NH * DH;
constexpr int NSW = 4, NTHR = 512;
constexpr int NT2 = 32, NTILE = 2 * NT2;
constexpr int NBK = 5;
constexpr int NJ = 56;
constexpr int EP_OFF = 0;
constexpr int A_OFF = 2048;
constexpr int ML_OFF = 4096;
constexpr int H_OFF = 8192;
constexpr int F_OFF = 32768;
constexpr int C_OFF = 49152;
constexpr int Q_OFF = 65536;
constexpr int P_OFF = Q_OFF + NB * NH * 1024;
constexpr int Y_OFF = P_OFF + NB * NH * 8192;
constexpr int Z_OFF = Y_OFF + NB * NJ * 1024;
constexpr int WS_USED = Z_OFF + NB * NJ * 4096;
#define PACE 438
constexpr int PACE0 = 445, POFF = 160, PMUL = 3;
constexpr int W_T0 = 900, W_DT = 88;
constexpr int D_A = 40, D_Y = 150;
constexpr int D_Q = 40, P_Q = 16, D_H = 15, D_Z = 13;
constexpr int CK_TH = 8, CK_TH2 = 80, CK_CAP = 60, CK_DP = 12, CK_DP1 = 3, CK_MAX = 20, CK_R = 40;
constexpr unsigned SPIN_CAP = 1u << 22;
constexpr float L2E = 1.4426950408889634f;

__device__ __forceinline__ float bflo(unsigned w) { return __builtin_bit_cast(float, w << 16); }
__device__ __forceinline__ float bfhi(unsigned w) { return __builtin_bit_cast(float, w & 0xffff0000u); }
__device__ __forceinline__ unsigned pk2(float lo, float hi) {
  bf16x2 v = {(__bf16)lo, (__bf16)hi};
  return __builtin_bit_cast(unsigned, v);
}
__device__ __forceinline__ float rbf(float f) { return bflo(pk2(f, 0.f)); }
__device__ __forceinline__ float dot2(unsigned a, unsigned b, float c) {
  return __builtin_amdgcn_fdot2_f32_bf16(__builtin_bit_cast(bf16x2, a), __builtin_bit_cast(bf16x2, b), c, false);
}
template <int CTRL, int BANK> __device__ __forceinline__ float dppu(float old, float x) {
  return __builtin_bit_cast(float, __builtin_amdgcn_update_dpp(__builtin_bit_cast(int, old), __builtin_bit_cast(int, x), CTRL, 0xf, BANK, false));
}
template <int CTRL> __device__ __forceinline__ float dpp(float x) {
  return __builtin_bit_cast(float, __builtin_amdgcn_mov_dpp(__builtin_bit_cast(int, x), CTRL, 0xf, 0xf, true));
}
template <int CTRL> __device__ __forceinline__ unsigned dppi(unsigned x) { return (unsigned)__builtin_amdgcn_mov_dpp((int)x, CTRL, 0xf, 0xf, true); }
constexpr int XOR1 = 0xB1, XOR2 = 0x4E, XOR7 = 0x141, ROR8 = 0x128, ROR4 = 0x124, SHL4 = 0x104, SHR4 = 0x114, BCAST = 0x150;
__device__ __forceinline__ float tr32(float a, float b) {
  auto s = __builtin_amdgcn_permlane32_swap(__float_as_uint(a), __float_as_uint(b), false, false);
  return __uint_as_float(s[0]) + __uint_as_float(s[1]);
}
__device__ __forceinline__ float tr16(float a, float b) {
  auto s = __builtin_amdgcn_permlane16_swap(__float_as_uint(a), __float_as_uint(b), false, false);
  return __uint_as_float(s[0]) + __uint_as_float(s[1]);
}
__device__ __forceinline__ float wave_sum(float x) {
  x += dpp<XOR1>(x); x += dpp<XOR2>(x); x += dpp<XOR7>(x); x += dpp<ROR8>(x);
  x = tr16(x, x);
  return tr32(x, x);
}
__device__ __forceinline__ float rdl(float x, int l) { return __builtin_bit_cast(float, __builtin_amdgcn_readlane(__builtin_bit_cast(int, x), l)); }
__device__ __forceinline__ float ex2(float x) { return __builtin_amdgcn_exp2f(x); }
__device__ __forceinline__ unsigned perm_lo(unsigned a, unsigned b) { return __builtin_amdgcn_perm(b, a, 0x05040100u); }
__device__ __forceinline__ unsigned perm_hi(unsigned a, unsigned b) { return __builtin_amdgcn_perm(b, a, 0x07060302u); }
__device__ __forceinline__ void pair_rows(u32x4& a, u32x4& b) {
  const u32x4 o0 = {perm_lo(a[0], b[0]), perm_hi(a[0], b[0]), perm_lo(a[1], b[1]), perm_hi(a[1], b[1])};
  const u32x4 o1 = {perm_lo(a[2], b[2]), perm_hi(a[2], b[2]), perm_lo(a[3], b[3]), perm_hi(a[3], b[3])};
  a = o0; b = o1;
}
__device__ __forceinline__ void dot8(const u32x4& a, const u32x4& b, unsigned xp, float (&acc)[8]) {
#pragma unroll
  for (int c = 0; c < 4; ++c) { acc[c] = dot2(xp, a[c], acc[c]); acc[4 + c] = dot2(xp, b[c], acc[4 + c]); }
}
#define ZERO8(a) _Pragma("unroll") for (int c_ = 0; c_ < 8; ++c_) { a[c_] = 0.f; asm volatile("" : "+v"(a[c_])); }

__device__ __forceinline__ u32x4 layernorm_pk(u32x4 xv, bool live) {
  float s1 = dot2(xv[0], 0x3f803f80u, 0.f), s2 = dot2(xv[0], xv[0], 0.f);
#pragma unroll
  for (int i = 1; i < 4; ++i) { s1 = dot2(xv[i], 0x3f803f80u, s1); s2 = dot2(xv[i], xv[i], s2); }
  s1 += dpp<XOR1>(s1); s2 += dpp<XOR1>(s2);
  s1 += dpp<XOR2>(s1); s2 += dpp<XOR2>(s2);
  s1 += dpp<XOR7>(s1); s2 += dpp<XOR7>(s2);
  s1 += dpp<ROR8>(s1); s2 += dpp<ROR8>(s2);
  const float t = tr16(s1, s2);
  float v[8];
#pragma unroll
  for (int i = 0; i < 4; ++i) { v[2 * i] = bflo(xv[i]); v[2 * i + 1] = bfhi(xv[i]); }
  const float t2 = tr32(t, t);
  const float mean = rdl(t2, 0) * (1.0f / DM), m2 = mean * mean;
  float var = fmaf(rdl(t2, 16), 1.0f / DM, -m2);
  if (__builtin_amdgcn_readfirstlane(!(m2 <= var))) {
    float q = 0.f;
#pragma unroll
    for (int i = 0; i < 8; ++i) { const float d = v[i] - mean; q = fmaf(d, d, q); }
    var = wave_sum(live ? q : 0.f) * (1.0f / DM);
  }
  const float mb = rbf(mean);
  const float sd = rbf(__builtin_amdgcn_sqrtf(rbf(rbf(var) + 1e-5f)));
  const float rs = __builtin_amdgcn_rcpf(sd);
  u32x4 o;
#pragma unroll
  for (int i = 0; i < 4; ++i) {
    unsigned d = pk2(v[2 * i] - mb, v[2 * i + 1] - mb);
    asm("" : "+v"(d));
    o[i] = pk2(bflo(d) * rs, bfhi(d) * rs);
  }
  return o;
}

#define MKRS(p, bytes) __builtin_amdgcn_make_buffer_rsrc((void*)(p), 0, (bytes), 0x00020000)
#define LD_NT(rs, voff, soff) __builtin_amdgcn_raw_buffer_load_b128(rs, voff, soff, 2)
#define LD_DEF(rs, voff, soff) __builtin_amdgcn_raw_buffer_load_b128(rs, voff, soff, 0)
#define LD_SC1(rs, voff, soff) __builtin_amdgcn_raw_buffer_load_b128(rs, voff, soff, 16)
#define LD2_SC1(rs, voff, soff) __builtin_amdgcn_raw_buffer_load_b64(rs, voff, soff, 16)
#define ST4_SC1(v, rs, voff, soff) __builtin_amdgcn_raw_buffer_store_b128(v, rs, voff, soff, 16)
#define ST2_SC1(v, rs, voff, soff) __builtin_amdgcn_raw_buffer_store_b64(v, rs, voff, soff, 16)
#define OOB 0x7ffffff0
#define CBAR() asm volatile("" ::: "memory")
#define WG_BARRIER() do { asm volatile("s_waitcnt lgkmcnt(0)" ::: "memory"); __builtin_amdgcn_s_barrier(); asm volatile("" ::: "memory"); } while (0)
#define LDS_WAIT() asm volatile("s_waitcnt lgkmcnt(0)" ::: "memory")
#define LVU(p) (*(volatile __attribute__((address_space(3))) unsigned*)(p))
#define PIN(x) asm volatile("" : "+v"(x))

struct Tile { u32x4 D[8]; };
#define TLD(rs, i) LD_NT(rs, voff + (((i) & 3) << 10) + (((i) >> 2) << 16), soff)
template <bool PACED>
__device__ __forceinline__ void tile_issue(Tile& T, rsrc_t rs, int voff, unsigned soff) {
#pragma unroll
  for (int i = 0; i < 8; ++i) { if (PACED) asm volatile("s_waitcnt vmcnt(7)" ::: "memory"); T.D[i] = TLD(rs, i); CBAR(); }
}

template <bool REFILL>
__device__ __forceinline__ void k_step(Tile& T, float& mx, float* sl, const u32x4& qv, int lane, rsrc_t rs, int voff, unsigned soff) {
  float x[8];
#pragma unroll
  for (int i = 0; i < 8; ++i) {
    float d = dot2(T.D[i][0], qv[0], 0.f);
    d = dot2(T.D[i][1], qv[1], d); d = dot2(T.D[i][2], qv[2], d); d = dot2(T.D[i][3], qv[3], d);
    x[i] = d;
    asm volatile("" : "+v"(x[i]) :: "memory");
    if (REFILL) T.D[i] = TLD(rs, i);
    __builtin_amdgcn_sched_barrier(0);
  }
#pragma unroll
  for (int i = 0; i < 4; ++i) x[i] = dppu<ROR8, 0xc>(x[i], x[i + 4]) + dppu<ROR8, 0x3>(x[i + 4], x[i]);
#pragma unroll
  for (int i = 0; i < 2; ++i) x[i] = dppu<XOR7, 0xa>(x[i], x[i + 2]) + dppu<XOR7, 0x5>(x[i + 2], x[i]);
  const float t0 = x[0] + dpp<XOR2>(x[0]), t1 = x[1] + dpp<XOR2>(x[1]);
  float sc = (lane & 2) ? t1 : t0;
  sc += dpp<XOR1>(sc);
  sc = tr16(sc, sc);
  sc = rbf(sc) * 0.0625f;
  mx = fmaxf(mx, sc);
  if ((lane & 17) == 0) sl[(lane >> 5) * 8 + ((lane & 15) >> 1)] = sc;
}

template <bool REFILL>
__device__ __forceinline__ void v_step(Tile& T, float (&acc)[8], const float* pl, int lane, rsrc_t rs, int voff, unsigned soff) {
  const f32x4 pa = *reinterpret_cast<const f32x4*>(pl + (lane >> 5) * 8), pb = *reinterpret_cast<const f32x4*>(pl + (lane >> 5) * 8 + 4);
#pragma unroll
  for (int i = 0; i < 8; ++i) {
    const float pi = i < 4 ? pa[i & 3] : pb[i & 3];
#pragma unroll
    for (int j = 0; j < 4; ++j) { acc[2 * j] = fmaf(pi, bflo(T.D[i][j]), acc[2 * j]); acc[2 * j + 1] = fmaf(pi, bfhi(T.D[i][j]), acc[2 * j + 1]); }
    asm volatile("" : "+v"(acc[0]), "+v"(acc[1]), "+v"(acc[2]), "+v"(acc[3]), "+v"(acc[4]), "+v"(acc[5]), "+v"(acc[6]), "+v"(acc[7]) :: "memory");
    if (REFILL) T.D[i] = TLD(rs, i);
    __builtin_amdgcn_sched_barrier(0);
  }
}

struct __attribute__((aligned(16))) Lds {
  u32x4 w1s[4][8][64];
  float sbuf[NSW][512];
  float zp[4][DM];
  float yp[4][112];
  unsigned xnp[4][32][8];
  unsigned qpk[128];
  float rec[NSW][DH + 4];
  float fs[4];
  unsigned kdone[4];
  unsigned late[4];
  unsigned tag, tag0;
  int dadj, cks, ckp;
  unsigned pad[3];
};
__device__ __forceinline__ unsigned now32() { return (unsigned)__builtin_amdgcn_s_memrealtime(); }

__global__ void __launch_bounds__(NTHR, 2)
fused_decode_kernel(const u16* __restrict__ x, const u16* __restrict__ kc, const u16* __restrict__ vc, const u16* __restrict__ wq,
                    const u16* __restrict__ wo, const u16* __restrict__ w1, const u16* __restrict__ w2, u16* out, unsigned char* ws) {
  __shared__ Lds L;
  const unsigned tref = now32();
  const int tid = threadIdx.x, lane = tid & 63, wv = __builtin_amdgcn_readfirstlane(tid >> 6);
  const int bid = blockIdx.x, xc = bid & 7, rr = bid >> 3;
  const int h = (rr & 1) * 8 + xc, sp = (rr >> 1) & 3, b = rr >> 3;
  const int loc = rr & 7, j = xc * 7 + loc;
  const bool tailwg = loc < 7;
  const rsrc_t rw = MKRS(ws, WS_USED);

  if (wv < NSW) {
    const int pair = b * NH + h;
    const size_t base = (size_t)pair * SK * DH;
    const rsrc_t rk = MKRS(kc + base, SK * DH * 2), rv = MKRS(vc + base, SK * DH * 2);
    const int voff = lane * 16;
    constexpr unsigned TS = 131072, RMASK = SK * DH * 2 - 1;
    unsigned soff = (wv * 4096 + sp * 16384 + pair * PMUL * TS) & RMASK;
    float* sw = L.sbuf[wv];
    Tile R, B[NBK];
    int gate = POFF * 64 + wv * (PACE * 4), gstep = PACE0 * 16;
    int sapp = 0, cks = 0, ckp = 0;
#define PACE_WAIT(MAIN) do { unsigned d_; for (;;) { d_ = now32() - tref; if (d_ > 1000000u || (int)(d_ * 64u) >= gate) break; __builtin_amdgcn_s_sleep(1); } \
      if (MAIN) { \
        if (lane == 0) LVU(&L.late[wv]) = (unsigned)((int)(d_ * 64u) - gate) >> 6; \
        const int a_ = min(cks - sapp, CK_R * 64); sapp += a_; gate += a_ + ckp; \
        cks = __builtin_amdgcn_readfirstlane((int)LVU(&L.cks)); ckp = __builtin_amdgcn_readfirstlane((int)LVU(&L.ckp)); } \
      gate += gstep; } while (0)
#define NEXT() (soff = (soff + TS) & RMASK)
    PACE_WAIT(false);
    tile_issue<false>(R, rk, voff, soff);
#pragma unroll
    for (int i = 0; i < NBK; ++i) { PACE_WAIT(false); NEXT(); tile_issue<true>(B[i], rk, voff, soff); }
    __builtin_amdgcn_sched_barrier(0);
    WG_BARRIER();
    const u32x4 qv = *reinterpret_cast<const u32x4*>(&L.qpk[(lane & 31) * 4]);
    float mx = -INFINITY;
    gstep = PACE * 16;
#pragma unroll
    for (int i = 0; i < NBK; ++i) {
      PACE_WAIT(false);
      NEXT();
      k_step<true>(R, mx, sw + (i ? NBK + i : 0) * 16, qv, lane, rk, voff, soff);
      k_step<false>(B[i], mx, sw + (i + 1) * 16, qv, lane, rk, voff, 0);
    }
    gstep += __builtin_amdgcn_readfirstlane((int)LVU(&L.dadj));
    for (int t = 2 * NBK + 1; t <= NT2; ++t) { PACE_WAIT(true); NEXT(); k_step<true>(R, mx, sw + (t - 1) * 16, qv, lane, t < NT2 ? rk : rv, voff, soff); }
    {
      float M = mx;
      M = fmaxf(M, dpp<XOR2>(M)); M = fmaxf(M, dpp<XOR7>(M)); M = fmaxf(M, dpp<ROR8>(M));
      auto sm = __builtin_amdgcn_permlane32_swap(__float_as_uint(M), __float_as_uint(M), false, false);
      M = fmaxf(__uint_as_float(sm[0]), __uint_as_float(sm[1]));
      LDS_WAIT();
      f32x4 s0 = *reinterpret_cast<const f32x4*>(sw + lane * 8), s1 = *reinterpret_cast<const f32x4*>(sw + lane * 8 + 4);
#pragma unroll
      for (int i = 0; i < 4; ++i) { s0[i] = ex2((s0[i] - M) * L2E); s1[i] = ex2((s1[i] - M) * L2E); }
      *reinterpret_cast<f32x4*>(sw + lane * 8) = s0; *reinterpret_cast<f32x4*>(sw + lane * 8 + 4) = s1;
      const float l = wave_sum(((s0[0] + s0[1]) + (s0[2] + s0[3])) + ((s1[0] + s1[1]) + (s1[2] + s1[3])));
      if (lane == 0) *reinterpret_cast<float2*>(L.rec[wv]) = make_float2(M, l);
      LDS_WAIT();
      if (lane == 0) LVU(&L.kdone[wv]) = 1u;
    }
    float acc[8];
#pragma unroll
    for (int i = 0; i < 8; ++i) acc[i] = 0.f;
    for (int t = NT2 + 1; t < NTILE; ++t) { PACE_WAIT(true); NEXT(); v_step<true>(R, acc, sw + (t - NT2 - 1) * 16, lane, rv, voff, soff); }
    v_step<false>(R, acc, sw + (NT2 - 1) * 16, lane, rv, voff, 0);
    {
      f32x4 o;
#pragma unroll
      for (int i = 0; i < 4; ++i) o[i] = tr32(acc[i], acc[i + 4]);
      *reinterpret_cast<f32x4*>(&L.rec[wv][4 + (lane & 31) * 8 + (lane >> 5) * 4]) = o;
    }
    const unsigned tag = __builtin_amdgcn_readfirstlane(L.tag);
    WG_BARRIER();
    if (wv == 0) {
      const u32x2 g = {now32(), tag};
      ST2_SC1(g, rw, lane < 8 ? lane * 512 : OOB, F_OFF + b * 4096 + (h * 4 + sp) * 8);
    }
    return;
  }

  const int hw = wv - NSW;
  if (hw == 2) {
    const size_t base = (size_t)(b * NH + h) * SK * DH;
    const rsrc_t rk = MKRS(kc + base, SK * DH * 2), rv = MKRS(vc + base, SK * DH * 2);
    const rsrc_t ro = MKRS(wo + (size_t)h * DH * DM, DH * DM * 2), r1 = MKRS(w1, DM * FF * 2), r2 = MKRS(w2, DM * FF * 2), rout = MKRS(out, NB * DM * 2);
    const int vt = lane < 2 ? lane * (2 << 20) : OOB;
    unsigned a = __builtin_amdgcn_raw_buffer_load_b32(rk, vt, 0, 2) + __builtin_amdgcn_raw_buffer_load_b32(rv, vt, 0, 2);
    a += __builtin_amdgcn_raw_buffer_load_b32(ro, rr < 2 ? lane * 65536 : OOB, 0, 2);
    a += __builtin_amdgcn_raw_buffer_load_b32(r1, (rr * 64 + lane) * 65536, 0, 2);
    a += __builtin_amdgcn_raw_buffer_load_b32(r2, (rr == 2 && lane < 4) ? lane * 65536 : OOB, xc * (7 * 32 * DM * 2), 2);
    a += __builtin_amdgcn_raw_buffer_load_b32(rout, lane == 0 ? 0 : OOB, 0, 2);
    PIN(a);
  }
  if (hw == 0) {
    const rsrc_t rx = MKRS(x, NB * DM * 2);
    const int vx = lane < 56 ? lane * 16 : OOB;
    u32x4 xv[4], W[14];
#pragma unroll
    for (int i = 0; i < 4; ++i) xv[i] = LD_DEF(rx, vx, i * (DM * 2));
    const unsigned ep = __builtin_amdgcn_raw_buffer_load_b32(rw, (lane >> 4) * 256, EP_OFF, 16);
    const rsrc_t rq = MKRS(wq + h * DH + (b * 4 + sp) * 16, (DM - 1) * HD * 2 + 32);
    const int vq = (lane >> 1) * (2 * HD * 2) + (lane & 1) * 16;
#pragma unroll
    for (int i = 0; i < 14; ++i) W[i] = LD_NT(rq, vq, ((i >> 1) * 64 + (i & 1)) * (HD * 2));
    CBAR();
    __builtin_amdgcn_sched_barrier(0);
#pragma unroll
    for (int i = 0; i < 4; ++i) {
      const u32x4 xn = layernorm_pk(xv[i], lane < 56);
      if (lane < 56) {
#pragma unroll
        for (int r = 0; r < 4; ++r) L.xnp[i][(lane & 7) * 4 + r][lane >> 3] = xn[r];
      }
    }
    const unsigned tagl = ep + 1u;
    const unsigned tg = __builtin_amdgcn_readlane((int)tagl, b * 16), tag0 = __builtin_amdgcn_readlane((int)tagl, 0);
    if (lane == 0) { L.tag = tg; L.tag0 = tag0; L.dadj = 0; L.cks = 0; L.ckp = 0; }
    if (lane < 4) { L.kdone[lane] = 0u; L.late[lane] = 0u; }
    { const u32x2 g = {tref, tag0}; ST2_SC1(g, rw, (rr == 0 && lane == 0) ? xc * 8 : OOB, A_OFF); }
    LDS_WAIT();
#pragma unroll
    for (int k = 0; k < 7; ++k) pair_rows(W[2 * k], W[2 * k + 1]);
    float acc[4][8];
#pragma unroll
    for (int i = 0; i < 4; ++i) {
      const u32x4 xa = *reinterpret_cast<const u32x4*>(&L.xnp[i][lane >> 1][0]);
      const u32x4 xb = *reinterpret_cast<const u32x4*>(&L.xnp[i][lane >> 1][4]);
#pragma unroll
      for (int c = 0; c < 8; ++c) acc[i][c] = 0.f;
#pragma unroll
      for (int k = 0; k < 7; ++k) dot8(W[2 * k], W[2 * k + 1], k < 4 ? xa[k & 3] : xb[k & 3], acc[i]);
    }
#pragma unroll
    for (int c = 0; c < 8; ++c) { acc[0][c] = tr32(acc[0][c], acc[2][c]); acc[1][c] = tr32(acc[1][c], acc[3][c]); }
#pragma unroll
    for (int c = 0; c < 8; ++c) acc[0][c] = tr16(acc[0][c], acc[1][c]);
    float* a = acc[0];
#pragma unroll
    for (int c = 0; c < 4; ++c) a[c] = dppu<ROR8, 0xc>(a[c], a[c + 4]) + dppu<ROR8, 0x3>(a[c + 4], a[c]);
#pragma unroll
    for (int c = 0; c < 2; ++c) a[c] = dppu<SHR4, 0xa>(a[c], a[c + 2]) + dppu<SHL4, 0x5>(a[c + 2], a[c]);
    a[0] += dpp<XOR2>(a[0]); a[1] += dpp<XOR2>(a[1]);
    {
      const u32x2 g = {pk2(a[0], a[1]), tagl};
      const int slot = (lane & 1) * 4 + ((lane >> 2) & 3);
      ST2_SC1(g, rw, (lane & 2) == 0 ? (lane >> 4) * (NH * 1024) + slot * 8 : OOB, Q_OFF + h * 1024 + (b * 4 + sp) * 64);
    }
    __builtin_amdgcn_s_sleep(D_Q);
    u32x4 G;
#pragma nounroll
    for (unsigned spin = 0; spin < SPIN_CAP; ++spin) {
      CBAR();
      G = LD_SC1(rw, lane * 16, Q_OFF + (b * NH + h) * 1024);
      if (__all((G[1] == tg) & (G[3] == tg))) break;
      __builtin_amdgcn_s_sleep(P_Q);
    }
    *reinterpret_cast<u32x2*>(&L.qpk[lane * 2]) = u32x2{G[0], G[2]};
  }
  WG_BARRIER();
  const unsigned tag = __builtin_amdgcn_readfirstlane(L.tag);
  if (hw == 1) {
    const u32x2 A = LD2_SC1(rw, (lane & 7) * 8, A_OFF);
    int d = (int)(A[0] - tref);
    const bool ok = __all(A[1] == L.tag0 && d > -700 && d < 700);
    d += (int)dppi<XOR1>((unsigned)d); d += (int)dppi<XOR2>((unsigned)d); d += (int)dppi<XOR7>((unsigned)d);
    if (lane == 0 && ok) LVU(&L.dadj) = (unsigned)((d * 8) / (NTILE - 2 * NBK - 2));
  }

#define CK_PUB(k) ((k) >= 2 && (k) % 3 == 2)
#define W_WAIT(k) do { \
    while ((int)(now32() - tref - (unsigned)(W_T0 + ((k) * 4 + hw) * W_DT + ((k) < 8 ? (bid & 15) * W_DT / 16 : (bid & 7) * W_DT / 8))) < 0) __builtin_amdgcn_s_sleep(2); \
    if (hw == 1 && CK_PUB(k)) { \
      unsigned lt_ = LVU(&L.late[lane & 3]); lt_ = max(lt_, dppi<XOR1>(lt_)); lt_ = max(lt_, dppi<XOR2>(lt_)); \
      const u32x2 g_ = {lt_, L.tag0}; \
      ST2_SC1(g_, rw, ((rr & 15) == 9 && lane == 0) ? (xc + (rr >> 4) * 8) * 8 : OOB, C_OFF + (k) * 128); } \
    if (hw == 1 && (k) > 0 && CK_PUB((k) - 1)) { \
      _Pragma("nounroll") for (int tr_ = 0; tr_ < 4; ++tr_) { \
        CBAR(); \
        const u32x2 A_ = LD2_SC1(rw, (lane & 15) * 8, C_OFF + ((k) - 1) * 128); \
        unsigned m_ = min(A_[0], 1000u); m_ += dppi<XOR1>(m_); m_ += dppi<XOR2>(m_); m_ += dppi<XOR7>(m_); m_ += dppi<ROR8>(m_); \
        if (__all(A_[1] == L.tag0)) { \
          if (lane == 0 && m_ > (unsigned)CK_TH * 16) { \
            LVU(&L.cks) = (unsigned)(L.cks + min((int)m_ * 8, (int)m_ * 4 + CK_CAP * 64)); \
            LVU(&L.ckp) = (unsigned)min(L.ckp + (m_ > (unsigned)CK_TH2 * 16 ? CK_DP1 : CK_DP) * 16, CK_MAX * 16); } \
          break; } \
        __builtin_amdgcn_s_sleep(40); } } \
  } while (0)
  u32x4 WO[16], W2[8];
  {
    const rsrc_t ro = MKRS(wo + (size_t)(h * DH + hw * 64) * DM + sp * 112, 63 * DM * 2 + 224);
    const int vo = (lane & 15) < 14 ? (lane >> 4) * 1792 + (lane & 15) * 16 : OOB;
#pragma unroll
    for (int k = 0; k < 8; ++k) {
      W_WAIT(k);
      WO[2 * k] = LD_NT(ro, vo, k * 7168); WO[2 * k + 1] = LD_NT(ro, vo + 896, k * 7168);
      pair_rows(WO[2 * k], WO[2 * k + 1]);
      PIN(WO[2 * k]); PIN(WO[2 * k + 1]);
    }
    if (hw == 0) {
#pragma nounroll
      for (unsigned spin = 0; spin < SPIN_CAP; ++spin) {
        if (__all(LVU(&L.kdone[lane & 3]) != 0u)) break;
        __builtin_amdgcn_s_sleep(8);
      }
      CBAR();
      const float2 ml = *reinterpret_cast<const float2*>(L.rec[lane & 3]);
      float M = fmaxf(ml.x, dpp<XOR1>(ml.x)); M = fmaxf(M, dpp<XOR2>(M));
      float Ls = ex2((ml.x - M) * L2E) * ml.y; Ls += dpp<XOR1>(Ls); Ls += dpp<XOR2>(Ls);
      const u32x4 g2 = {__float_as_uint(M), tag, __float_as_uint(Ls), tag};
      ST4_SC1(g2, rw, lane == 0 ? 0 : OOB, ML_OFF + (b * 64 + h * 4 + sp) * 16);
      u32x4 ML;
#pragma nounroll
      for (unsigned spin = 0; spin < SPIN_CAP; ++spin) {
        __builtin_amdgcn_s_sleep(64);
        CBAR();
        ML = LD_SC1(rw, (lane & 3) * 16, ML_OFF + (b * 64 + h * 4) * 16);
        if (__all((ML[1] == tag) & (ML[3] == tag))) break;
      }
      const float ms = __uint_as_float(ML[0]);
      float Mh = fmaxf(ms, dpp<XOR1>(ms)); Mh = fmaxf(Mh, dpp<XOR2>(Mh));
      float Lh = ex2((ms - Mh) * L2E) * __uint_as_float(ML[2]); Lh += dpp<XOR1>(Lh); Lh += dpp<XOR2>(Lh);
      if (lane < 4) L.fs[lane] = ex2((ml.x - Mh) * L2E) / Lh;
    }
    const rsrc_t r1 = MKRS(w1, tailwg ? DM * FF * 2 : 0);
    const int r16 = lane >> 2;
    const int v1 = r16 * (2 * FF * 2) + (lane & 3) * 16 + j * 64;
#pragma unroll
    for (int i = 0; i < 4; ++i) {
      W_WAIT(8 + i);
      u32x4 a = LD_NT(r1, v1, (hw * 4 + i) * (32 * FF * 2)), c = LD_NT(r1, v1 + FF * 2, (hw * 4 + i) * (32 * FF * 2));
      pair_rows(a, c);
      L.w1s[lane & 3][(r16 & 3) * 2][4 * (hw * 4 + i) + (r16 >> 2)] = a; L.w1s[lane & 3][(r16 & 3) * 2 + 1][4 * (hw * 4 + i) + (r16 >> 2)] = c;
      CBAR();
    }
    const rsrc_t r2 = MKRS(w2 + (size_t)((tailwg ? j : 0) * 32 + hw * 8) * DM, tailwg ? 8 * DM * 2 : 0);
    const int v2 = lane < 56 ? lane * 16 : OOB;
#pragma unroll
    for (int i = 0; i < 4; ++i) {
      W_WAIT(12 + i);
      W2[2 * i] = LD_NT(r2, v2, i * 1792); W2[2 * i + 1] = LD_NT(r2, v2, i * 1792 + 896);
      pair_rows(W2[2 * i], W2[2 * i + 1]); PIN(W2[2 * i]); PIN(W2[2 * i + 1]);
    }
  }
  unsigned xw = *reinterpret_cast<const unsigned*>(x + b * DM + 8 * (tailwg ? j : 0) + (lane & 3) * 2);
  PIN(xw);
  const bool live = lane < 56;
  const int dsel = hw * 64 + ((lane >> 1) & 7) * 8 + ((lane >> 5) + ((lane >> 4) & 1) * 2) * 2 + (lane & 1);
  __builtin_amdgcn_sched_barrier(0);
  WG_BARRIER();
  {
    const f32x4 fs = *reinterpret_cast<const f32x4*>(L.fs);
    float o = 0.f;
#pragma unroll
    for (int w = 0; w < NSW; ++w) o = fmaf(fs[w], L.rec[w][4 + dsel], o);
    const u32x2 g = {__float_as_uint(o), tag};
    ST2_SC1(g, rw, lane * 8, P_OFF + (b * NH + h) * 8192 + hw * 2048 + sp * 512);
  }
  const unsigned tpub = now32();
  __builtin_amdgcn_sched_barrier(0);
  u32x4 W1[8];
  {
#pragma unroll
    for (int i = 0; i < 8; ++i) W1[i] = L.w1s[hw][i][lane];
#pragma unroll
    for (int i = 0; i < 8; ++i) PIN(W1[i]);
    float acc[8];
    ZERO8(acc);
    while (now32() - tpub < (unsigned)D_A) __builtin_amdgcn_s_sleep(1);
    u32x4 P0, P1;
#pragma nounroll
    for (unsigned spin = 0; spin < SPIN_CAP; ++spin) {
      CBAR();
      P0 = LD_SC1(rw, lane * 16, P_OFF + (b * NH + h) * 8192 + hw * 2048);
      P1 = LD_SC1(rw, lane * 16 + 1024, P_OFF + (b * NH + h) * 8192 + hw * 2048);
      if (__all((P0[1] == tag) & (P0[3] == tag) & (P1[1] == tag) & (P1[3] == tag))) break;
    }
    float o0 = __uint_as_float(P0[0]) + __uint_as_float(P1[0]), o1 = __uint_as_float(P0[2]) + __uint_as_float(P1[2]);
    o0 = tr32(o0, o0); o1 = tr32(o1, o1);
    unsigned op = pk2(o0, o1);
    op = (unsigned)__builtin_amdgcn_update_dpp((int)op, (int)op, ROR8, 0xc, 0xf, false);
#define YK(k) dot8(WO[2 * k], WO[2 * k + 1], dppi<BCAST + k>(op), acc);
    YK(0) YK(1) YK(2) YK(3) YK(4) YK(5) YK(6) YK(7)
#undef YK
#pragma unroll
    for (int c = 0; c < 4; ++c) acc[c] = tr32(acc[c], acc[c + 4]);
#pragma unroll
    for (int c = 0; c < 2; ++c) acc[c] = tr16(acc[c], acc[c + 2]);
    if ((lane & 15) < 14) *reinterpret_cast<float2*>(&L.yp[hw][(lane & 15) * 8 + (lane >> 4) * 2]) = make_float2(acc[0], acc[1]);
    WG_BARRIER();
    if (hw == 1) {
      const int c2 = (live ? lane : 0) * 2;
      const float2 u0 = *reinterpret_cast<const float2*>(&L.yp[0][c2]), u1 = *reinterpret_cast<const float2*>(&L.yp[1][c2]);
      const float2 u2 = *reinterpret_cast<const float2*>(&L.yp[2][c2]), u3 = *reinterpret_cast<const float2*>(&L.yp[3][c2]);
      const u32x4 g = {__float_as_uint((u0.x + u1.x) + (u2.x + u3.x)), tag, __float_as_uint((u0.y + u1.y) + (u2.y + u3.y)), tag};
      ST4_SC1(g, rw, live ? (lane >> 2) * 1024 + (lane & 3) * 16 : OOB, Y_OFF + (b * NJ + sp * 14) * 1024 + h * 64);
    }
  }
  if (!tailwg) return;

  unsigned hp = 0;
  float acc[8], z[8];
  ZERO8(acc); ZERO8(z);
  const int col = hw * 112 + (live ? lane : 0) * 2;
  int zoff = live ? (col >> 3) * 4096 + (col & 7) * 8 : OOB;
  PIN(zoff);
  if (hw == 0) {
    u32x2 F;
#pragma nounroll
    for (unsigned spin = 0; spin < SPIN_CAP; ++spin) {
      CBAR();
      F = LD2_SC1(rw, lane * 8, F_OFF + b * 4096 + xc * 512);
      if (__all(F[1] == tag)) break;
    }
#pragma nounroll
    for (unsigned spin = 0; spin < 4096; ++spin) {
      if (__all(now32() - F[0] >= (unsigned)D_Y)) break;
    }
    u32x4 Y;
#pragma nounroll
    for (unsigned spin = 0; spin < SPIN_CAP; ++spin) {
      CBAR();
      Y = LD_SC1(rw, lane * 16, Y_OFF + (b * NJ + j) * 1024);
      if (__all((Y[1] == tag) & (Y[3] == tag))) break;
    }
    float a0 = __uint_as_float(Y[0]), a1 = __uint_as_float(Y[2]);
    a0 += dpp<ROR8>(a0); a1 += dpp<ROR8>(a1);
    a0 += dpp<ROR4>(a0); a1 += dpp<ROR4>(a1);
    a0 = tr16(a0, a0); a1 = tr16(a1, a1);
    a0 = tr32(a0, a0); a1 = tr32(a1, a1);
    const unsigned ap = pk2(a0, a1);
    hp = pk2(bflo(xw) + bflo(ap), bfhi(xw) + bfhi(ap));
    const u32x2 g = {hp, tag};
    ST2_SC1(g, rw, lane < 4 ? lane * 8 : OOB, H_OFF + b * 2048 + j * 32);
  }
  WG_BARRIER();
  __builtin_amdgcn_s_sleep(D_H);
  u32x4 G0, G1;
  const int goff = live ? lane * 32 : OOB;
#pragma nounroll
  for (unsigned spin = 0; spin < SPIN_CAP; ++spin) {
    CBAR();
    G0 = LD_SC1(rw, goff, H_OFF + b * 2048);
    G1 = LD_SC1(rw, goff, H_OFF + b * 2048 + 16);
    const bool ok = ((G0[1] == tag) & (G0[3] == tag) & (G1[1] == tag) & (G1[3] == tag)) | !live;
    if (__all(ok)) break;
  }
  const u32x4 hn = layernorm_pk(u32x4{G0[0], G0[2], G1[0], G1[2]}, live);
#pragma unroll
  for (int k = 0; k < 4; ++k) dot8(W1[2 * k], W1[2 * k + 1], hn[k], acc);
#pragma unroll
  for (int c = 0; c < 4; ++c) acc[c] = tr32(acc[c], acc[c + 4]);
#pragma unroll
  for (int c = 0; c < 2; ++c) acc[c] = tr16(acc[c], acc[c + 2]);
  float t0 = acc[0], t1 = acc[1];
  t0 += dpp<ROR8>(t0); PIN(t0); t1 += dpp<ROR8>(t1); PIN(t1);
  t0 += dpp<ROR4>(t0); PIN(t0); t1 += dpp<ROR4>(t1); PIN(t1);
  t0 += dpp<XOR2>(t0); PIN(t0); t1 += dpp<XOR2>(t1); PIN(t1);
  t0 += dpp<XOR1>(t0); PIN(t0); t1 += dpp<XOR1>(t1); PIN(t1);
  const unsigned tp = pk2(t0, t1);
  t0 = bflo(tp); t1 = bfhi(tp);
  const float s0 = t0 * __builtin_amdgcn_rcpf(1.0f + ex2(-t0 * L2E)), s1 = t1 * __builtin_amdgcn_rcpf(1.0f + ex2(-t1 * L2E));
  const unsigned sp2 = pk2(s0, s1);
#pragma unroll
  for (int k = 0; k < 4; ++k) dot8(W2[2 * k], W2[2 * k + 1], (unsigned)__builtin_amdgcn_readlane((int)sp2, 16 * k), z);
  if (live) {
    *reinterpret_cast<f32x4*>(&L.zp[hw][lane * 8]) = f32x4{z[0], z[1], z[2], z[3]};
    *reinterpret_cast<f32x4*>(&L.zp[hw][lane * 8 + 4]) = f32x4{z[4], z[5], z[6], z[7]};
  }
  WG_BARRIER();
  {
    const float2 u0 = *reinterpret_cast<const float2*>(&L.zp[0][col]), u1 = *reinterpret_cast<const float2*>(&L.zp[1][col]);
    const float2 u2 = *reinterpret_cast<const float2*>(&L.zp[2][col]), u3 = *reinterpret_cast<const float2*>(&L.zp[3][col]);
    const u32x4 g = {__float_as_uint((u0.x + u1.x) + (u2.x + u3.x)), tag, __float_as_uint((u0.y + u1.y) + (u2.y + u3.y)), tag};
    ST4_SC1(g, rw, zoff, Z_OFF + b * NJ * 4096 + j * 64);
  }
  if (hw == 0) {
    const rsrc_t rout = MKRS(out + b * DM + 8 * j, 16);
    const int z3 = lane < 32 ? lane * 16 + 3072 : OOB;
    __builtin_amdgcn_s_sleep(D_Z);
    u32x4 Z[4];
#pragma nounroll
    for (unsigned spin = 0; spin < SPIN_CAP; ++spin) {
      CBAR();
#pragma unroll
      for (int i = 0; i < 3; ++i) Z[i] = LD_SC1(rw, lane * 16 + i * 1024, Z_OFF + (b * NJ + j) * 4096);
      Z[3] = LD_SC1(rw, z3, Z_OFF + (b * NJ + j) * 4096);
      bool ok = ((Z[3][1] == tag) & (Z[3][3] == tag)) | (lane >= 32);
#pragma unroll
      for (int i = 0; i < 3; ++i) ok &= (Z[i][1] == tag) & (Z[i][3] == tag);
      if (__all(ok)) break;
    }
    float a0 = (__uint_as_float(Z[0][0]) + __uint_as_float(Z[1][0])) + (__uint_as_float(Z[2][0]) + __uint_as_float(Z[3][0]));
    float a1 = (__uint_as_float(Z[0][2]) + __uint_as_float(Z[1][2])) + (__uint_as_float(Z[2][2]) + __uint_as_float(Z[3][2]));
    a0 += dpp<ROR8>(a0); a1 += dpp<ROR8>(a1);
    a0 += dpp<ROR4>(a0); a1 += dpp<ROR4>(a1);
    a0 = tr16(a0, a0); a1 = tr16(a1, a1);
    a0 = tr32(a0, a0); a1 = tr32(a1, a1);
    const unsigned mp = pk2(a0, a1);
    const unsigned op = pk2(bflo(hp) + bflo(mp), bfhi(hp) + bfhi(mp));
    __builtin_amdgcn_raw_buffer_store_b32(op, rout, lane * 4, 0, 0);
    __builtin_amdgcn_raw_buffer_store_b32(tag, rw, (j == 0 && lane == 0) ? 0 : OOB, EP_OFF + b * 256, 16);
  }
}

extern "C" void kernel_launch(void* const* d_in, const int* in_sizes, int n_in, void* d_out, int out_size, void* d_ws, size_t ws_size, hipStream_t stream) {
  fused_decode_kernel<<<NB * NH * 4, NTHR, 0, stream>>>((const u16*)d_in[0], (const u16*)d_in[1], (const u16*)d_in[2], (const u16*)d_in[3],
                                                       (const u16*)d_in[4], (const u16*)d_in[6], (const u16*)d_in[7], (u16*)d_out,
                                                       (unsigned char*)d_ws);
}
```

```cpp
#include <hip/hip_runtime.h>
#include <hip/hip_bf16.h>
#include <stdint.h>

typedef unsigned u32x4 __attribute__((ext_vector_type(4)));
typedef unsigned u32x2 __attribute__((ext_vector_type(2)));
typedef float f32x4 __attribute__((ext_vector_type(4)));
typedef __bf16 bf16x2 __attribute__((ext_vector_type(2)));
typedef unsigned short u16;
typedef __amdgpu_buffer_rsrc_t rsrc_t;

constexpr int NB = 4, DM = 448, NH = 16, SK = 8192, DH = 256, FF = 1792, HD = NH * DH;
constexpr int NSW = 4, NTHR = 512;
constexpr int NT2 = 32, NTILE = 2 * NT2;
constexpr int NBK = 5;
constexpr int NJ = 56;
constexpr int EP_OFF = 0;
constexpr int A_OFF = 2048;
constexpr int ML_OFF = 4096;
constexpr int H_OFF = 8192;
constexpr int F_OFF = 32768;
constexpr int C_OFF = 49152;
constexpr int Q_OFF = 65536;
constexpr int P_OFF = Q_OFF + NB * NH * 1024;
constexpr int Y_OFF = P_OFF + NB * NH * 8192;
constexpr int Z_OFF = Y_OFF + NB * NJ * 1024;
constexpr int WS_USED = Z_OFF + NB * NJ * 4096;
#define PACE 437
constexpr int PACE0 = 445, POFF = 160, PMUL = 3;
constexpr int W_T0 = 900, W_DT = 88;
constexpr int D_A = 40, D_Y = 150;
constexpr int D_Q = 40, P_Q = 16, D_H = 15, D_Z = 13;
constexpr int CK_TH = 8, CK_TH2 = 80, CK_CAP = 60, CK_DP = 12, CK_DP1 = 3, CK_MAX = 20, CK_R = 40;
constexpr unsigned SPIN_CAP = 1u << 22;
constexpr float L2E = 1.4426950408889634f;

__device__ __forceinline__ float bflo(unsigned w) { return __builtin_bit_cast(float, w << 16); }
__device__ __forceinline__ float bfhi(unsigned w) { return __builtin_bit_cast(float, w & 0xffff0000u); }
__device__ __forceinline__ unsigned pk2(float lo, float hi) {
  bf16x2 v = {(__bf16)lo, (__bf16)hi};
  return __builtin_bit_cast(unsigned, v);
}
__device__ __forceinline__ float rbf(float f) { return bflo(pk2(f, 0.f)); }
__device__ __forceinline__ float dot2(unsigned a, unsigned b, float c) {
  return __builtin_amdgcn_fdot2_f32_bf16(__builtin_bit_cast(bf16x2, a), __builtin_bit_cast(bf16x2, b), c, false);
}
template <int CTRL, int BANK> __device__ __forceinline__ float dppu(float old, float x) {
  return __builtin_bit_cast(float, __builtin_amdgcn_update_dpp(__builtin_bit_cast(int, old), __builtin_bit_cast(int, x), CTRL, 0xf, BANK, false));
}
template <int CTRL> __device__ __forceinline__ float dpp(float x) {
  return __builtin_bit_cast(float, __builtin_amdgcn_mov_dpp(__builtin_bit_cast(int, x), CTRL, 0xf, 0xf, true));
}
template <int CTRL> __device__ __forceinline__ unsigned dppi(unsigned x) { return (unsigned)__builtin_amdgcn_mov_dpp((int)x, CTRL, 0xf, 0xf, true); }
constexpr int XOR1 = 0xB1, XOR2 = 0x4E, XOR7 = 0x141, ROR8 = 0x128, ROR4 = 0x124, SHL4 = 0x104, SHR4 = 0x114, BCAST = 0x150;
__device__ __forceinline__ float tr32(float a, float b) {
  auto s = __builtin_amdgcn_permlane32_swap(__float_as_uint(a), __float_as_uint(b), false, false);
  return __uint_as_float(s[0]) + __uint_as_float(s[1]);
}
__device__ __forceinline__ float tr16(float a, float b) {
  auto s = __builtin_amdgcn_permlane16_swap(__float_as_uint(a), __float_as_uint(b), false, false);
  return __uint_as_float(s[0]) + __uint_as_float(s[1]);
}
__device__ __forceinline__ float wave_sum(float x) {
  x += dpp<XOR1>(x); x += dpp<XOR2>(x); x += dpp<XOR7>(x); x += dpp<ROR8>(x);
  x = tr16(x, x);
  return tr32(x, x);
}
__device__ __forceinline__ float rdl(float x, int l) { return __builtin_bit_cast(float, __builtin_amdgcn_readlane(__builtin_bit_cast(int, x), l)); }
__device__ __forceinline__ float ex2(float x) { return __builtin_amdgcn_exp2f(x); }
__device__ __forceinline__ unsigned perm_lo(unsigned a, unsigned b) { return __builtin_amdgcn_perm(b, a, 0x05040100u); }
__device__ __forceinline__ unsigned perm_hi(unsigned a, unsigned b) { return __builtin_amdgcn_perm(b, a, 0x07060302u); }
__device__ __forceinline__ void pair_rows(u32x4& a, u32x4& b) {
  const u32x4 o0 = {perm_lo(a[0], b[0]), perm_hi(a[0], b[0]), perm_lo(a[1], b[1]), perm_hi(a[1], b[1])};
  const u32x4 o1 = {perm_lo(a[2], b[2]), perm_hi(a[2], b[2]), perm_lo(a[3], b[3]), perm_hi(a[3], b[3])};
  a = o0; b = o1;
}
__device__ __forceinline__ void dot8(const u32x4& a, const u32x4& b, unsigned xp, float (&acc)[8]) {
#pragma unroll
  for (int c = 0; c < 4; ++c) { acc[c] = dot2(xp, a[c], acc[c]); acc[4 + c] = dot2(xp, b[c], acc[4 + c]); }
}
#define ZERO8(a) _Pragma("unroll") for (int c_ = 0; c_ < 8; ++c_) { a[c_] = 0.f; asm volatile("" : "+v"(a[c_])); }

__device__ __forceinline__ u32x4 layernorm_pk(u32x4 xv, bool live) {
  float s1 = dot2(xv[0], 0x3f803f80u, 0.f), s2 = dot2(xv[0], xv[0], 0.f);
#pragma unroll
  for (int i = 1; i < 4; ++i) { s1 = dot2(xv[i], 0x3f803f80u, s1); s2 = dot2(xv[i], xv[i], s2); }
  s1 += dpp<XOR1>(s1); s2 += dpp<XOR1>(s2);
  s1 += dpp<XOR2>(s1); s2 += dpp<XOR2>(s2);
  s1 += dpp<XOR7>(s1); s2 += dpp<XOR7>(s2);
  s1 += dpp<ROR8>(s1); s2 += dpp<ROR8>(s2);
  const float t = tr16(s1, s2);
  float v[8];
#pragma unroll
  for (int i = 0; i < 4; ++i) { v[2 * i] = bflo(xv[i]); v[2 * i + 1] = bfhi(xv[i]); }
  const float t2 = tr32(t, t);
  const float mean = rdl(t2, 0) * (1.0f / DM), m2 = mean * mean;
  float var = fmaf(rdl(t2, 16), 1.0f / DM, -m2);
  if (__builtin_amdgcn_readfirstlane(!(m2 <= var))) {
    float q = 0.f;
#pragma unroll
    for (int i = 0; i < 8; ++i) { const float d = v[i] - mean; q = fmaf(d, d, q); }
    var = wave_sum(live ? q : 0.f) * (1.0f / DM);
  }
  const float mb = rbf(mean);
  const float sd = rbf(__builtin_amdgcn_sqrtf(rbf(rbf(var) + 1e-5f)));
  const float rs = __builtin_amdgcn_rcpf(sd);
  u32x4 o;
#pragma unroll
  for (int i = 0; i < 4; ++i) {
    unsigned d = pk2(v[2 * i] - mb, v[2 * i + 1] - mb);
    asm("" : "+v"(d));
    o[i] = pk2(bflo(d) * rs, bfhi(d) * rs);
  }
  return o;
}

#define MKRS(p, bytes) __builtin_amdgcn_make_buffer_rsrc((void*)(p), 0, (bytes), 0x00020000)
#define LD_NT(rs, voff, soff) __builtin_amdgcn_raw_buffer_load_b128(rs, voff, soff, 2)
#define LD_DEF(rs, voff, soff) __builtin_amdgcn_raw_buffer_load_b128(rs, voff, soff, 0)
#define LD_SC1(rs, voff, soff) __builtin_amdgcn_raw_buffer_load_b128(rs, voff, soff, 16)
#define LD2_SC1(rs, voff, soff) __builtin_amdgcn_raw_buffer_load_b64(rs, voff, soff, 16)
#define ST4_SC1(v, rs, voff, soff) __builtin_amdgcn_raw_buffer_store_b128(v, rs, voff, soff, 16)
#define ST2_SC1(v, rs, voff, soff) __builtin_amdgcn_raw_buffer_store_b64(v, rs, voff, soff, 16)
#define OOB 0x7ffffff0
#define CBAR() asm volatile("" ::: "memory")
#define WG_BARRIER() do { asm volatile("s_waitcnt lgkmcnt(0)" ::: "memory"); __builtin_amdgcn_s_barrier(); asm volatile("" ::: "memory"); } while (0)
#define LDS_WAIT() asm volatile("s_waitcnt lgkmcnt(0)" ::: "memory")
#define LVU(p) (*(volatile __attribute__((address_space(3))) unsigned*)(p))
#define PIN(x) asm volatile("" : "+v"(x))

struct Tile { u32x4 D[8]; };
#define TLD(rs, i) LD_NT(rs, voff + (((i) & 3) << 10) + (((i) >> 2) << 16), soff)
template <bool PACED>
__device__ __forceinline__ void tile_issue(Tile& T, rsrc_t rs, int voff, unsigned soff) {
#pragma unroll
  for (int i = 0; i < 8; ++i) { if (PACED) asm volatile("s_waitcnt vmcnt(7)" ::: "memory"); T.D[i] = TLD(rs, i); CBAR(); }
}

template <bool REFILL>
__device__ __forceinline__ void k_step(Tile& T, float& mx, float* sl, const u32x4& qv, int lane, rsrc_t rs, int voff, unsigned soff) {
  float x[8];
#pragma unroll
  for (int i = 0; i < 8; ++i) {
    float d = dot2(T.D[i][0], qv[0], 0.f);
    d = dot2(T.D[i][1], qv[1], d); d = dot2(T.D[i][2], qv[2], d); d = dot2(T.D[i][3], qv[3], d);
    x[i] = d;
    asm volatile("" : "+v"(x[i]) :: "memory");
    if (REFILL) T.D[i] = TLD(rs, i);
    __builtin_amdgcn_sched_barrier(0);
  }
#pragma unroll
  for (int i = 0; i < 4; ++i) x[i] = dppu<ROR8, 0xc>(x[i], x[i + 4]) + dppu<ROR8, 0x3>(x[i + 4], x[i]);
#pragma unroll
  for (int i = 0; i < 2; ++i) x[i] = dppu<XOR7, 0xa>(x[i], x[i + 2]) + dppu<XOR7, 0x5>(x[i + 2], x[i]);
  const float t0 = x[0] + dpp<XOR2>(x[0]), t1 = x[1] + dpp<XOR2>(x[1]);
  float sc = (lane & 2) ? t1 : t0;
  sc += dpp<XOR1>(sc);
  sc = tr16(sc, sc);
  sc = rbf(sc) * 0.0625f;
  mx = fmaxf(mx, sc);
  if ((lane & 17) == 0) sl[(lane >> 5) * 8 + ((lane & 15) >> 1)] = sc;
}

template <bool REFILL>
__device__ __forceinline__ void v_step(Tile& T, float (&acc)[8], const float* pl, int lane, rsrc_t rs, int voff, unsigned soff) {
  const f32x4 pa = *reinterpret_cast<const f32x4*>(pl + (lane >> 5) * 8), pb = *reinterpret_cast<const f32x4*>(pl + (lane >> 5) * 8 + 4);
#pragma unroll
  for (int i = 0; i < 8; ++i) {
    const float pi = i < 4 ? pa[i & 3] : pb[i & 3];
#pragma unroll
    for (int j = 0; j < 4; ++j) { acc[2 * j] = fmaf(pi, bflo(T.D[i][j]), acc[2 * j]); acc[2 * j + 1] = fmaf(pi, bfhi(T.D[i][j]), acc[2 * j + 1]); }
    asm volatile("" : "+v"(acc[0]), "+v"(acc[1]), "+v"(acc[2]), "+v"(acc[3]), "+v"(acc[4]), "+v"(acc[5]), "+v"(acc[6]), "+v"(acc[7]) :: "memory");
    if (REFILL) T.D[i] = TLD(rs, i);
    __builtin_amdgcn_sched_barrier(0);
  }
}

struct __attribute__((aligned(16))) Lds {
  u32x4 w1s[4][8][64];
  float sbuf[NSW][512];
  float zp[4][DM];
  float yp[4][112];
  unsigned xnp[4][32][8];
  unsigned qpk[128];
  float rec[NSW][DH + 4];
  float fs[4];
  unsigned kdone[4];
  unsigned late[4];
  unsigned tag, tag0;
  int dadj, cks, ckp;
  unsigned pad[3];
};
__device__ __forceinline__ unsigned now32() { return (unsigned)__builtin_amdgcn_s_memrealtime(); }

__global__ void __launch_bounds__(NTHR, 2)
fused_decode_kernel(const u16* __restrict__ x, const u16* __restrict__ kc, const u16* __restrict__ vc, const u16* __restrict__ wq,
                    const u16* __restrict__ wo, const u16* __restrict__ w1, const u16* __restrict__ w2, u16* out, unsigned char* ws) {
  __shared__ Lds L;
  const unsigned tref = now32();
  const int tid = threadIdx.x, lane = tid & 63, wv = __builtin_amdgcn_readfirstlane(tid >> 6);
  const int bid = blockIdx.x, xc = bid & 7, rr = bid >> 3;
  const int h = (rr & 1) * 8 + xc, sp = (rr >> 1) & 3, b = rr >> 3;
  const int loc = rr & 7, j = xc * 7 + loc;
  const bool tailwg = loc < 7;
  const rsrc_t rw = MKRS(ws, WS_USED);

  if (wv < NSW) {
    const int pair = b * NH + h;
    const size_t base = (size_t)pair * SK * DH;
    const rsrc_t rk = MKRS(kc + base, SK * DH * 2), rv = MKRS(vc + base, SK * DH * 2);
    const int voff = lane * 16;
    constexpr unsigned TS = 131072, RMASK = SK * DH * 2 - 1;
    unsigned soff = (wv * 4096 + sp * 16384 + pair * PMUL * TS) & RMASK;
    float* sw = L.sbuf[wv];
    Tile R, B[NBK];
    int gate = POFF * 64 + wv * (PACE * 4), gstep = PACE0 * 16;
    int sapp = 0, cks = 0, ckp = 0;
#define PACE_WAIT(MAIN) do { unsigned d_; for (;;) { d_ = now32() - tref; if (d_ > 1000000u || (int)(d_ * 64u) >= gate) break; __builtin_amdgcn_s_sleep(1); } \
      if (MAIN) { \
        if (lane == 0) LVU(&L.late[wv]) = (unsigned)((int)(d_ * 64u) - gate) >> 6; \
        const int a_ = min(cks - sapp, CK_R * 64); sapp += a_; gate += a_ + ckp; \
        cks = __builtin_amdgcn_readfirstlane((int)LVU(&L.cks)); ckp = __builtin_amdgcn_readfirstlane((int)LVU(&L.ckp)); } \
      gate += gstep; } while (0)
#define NEXT() (soff = (soff + TS) & RMASK)
    PACE_WAIT(false);
    tile_issue<false>(R, rk, voff, soff);
#pragma unroll
    for (int i = 0; i < NBK; ++i) { PACE_WAIT(false); NEXT(); tile_issue<true>(B[i], rk, voff, soff); }
    __builtin_amdgcn_sched_barrier(0);
    WG_BARRIER();
    const u32x4 qv = *reinterpret_cast<const u32x4*>(&L.qpk[(lane & 31) * 4]);
    float mx = -INFINITY;
    gstep = PACE * 16;
#pragma unroll
    for (int i = 0; i < NBK; ++i) {
      PACE_WAIT(false);
      NEXT();
      k_step<true>(R, mx, sw + (i ? NBK + i : 0) * 16, qv, lane, rk, voff, soff);
      k_step<false>(B[i], mx, sw + (i + 1) * 16, qv, lane, rk, voff, 0);
    }
    gstep += __builtin_amdgcn_readfirstlane((int)LVU(&L.dadj));
    for (int t = 2 * NBK + 1; t <= NT2; ++t) { PACE_WAIT(true); NEXT(); k_step<true>(R, mx, sw + (t - 1) * 16, qv, lane, t < NT2 ? rk : rv, voff, soff); }
    {
      float M = mx;
      M = fmaxf(M, dpp<XOR2>(M)); M = fmaxf(M, dpp<XOR7>(M)); M = fmaxf(M, dpp<ROR8>(M));
      auto sm = __builtin_amdgcn_permlane32_swap(__float_as_uint(M), __float_as_uint(M), false, false);
      M = fmaxf(__uint_as_float(sm[0]), __uint_as_float(sm[1]));
      LDS_WAIT();
      f32x4 s0 = *reinterpret_cast<const f32x4*>(sw + lane * 8), s1 = *reinterpret_cast<const f32x4*>(sw + lane * 8 + 4);
#pragma unroll
      for (int i = 0; i < 4; ++i) { s0[i] = ex2((s0[i] - M) * L2E); s1[i] = ex2((s1[i] - M) * L2E); }
      *reinterpret_cast<f32x4*>(sw + lane * 8) = s0; *reinterpret_cast<f32x4*>(sw + lane * 8 + 4) = s1;
      const float l = wave_sum(((s0[0] + s0[1]) + (s0[2] + s0[3])) + ((s1[0] + s1[1]) + (s1[2] + s1[3])));
      if (lane == 0) *reinterpret_cast<float2*>(L.rec[wv]) = make_float2(M, l);
      LDS_WAIT();
      if (lane == 0) LVU(&L.kdone[wv]) = 1u;
    }
    float acc[8];
#pragma unroll
    for (int i = 0; i < 8; ++i) acc[i] = 0.f;
    for (int t = NT2 + 1; t < NTILE; ++t) { PACE_WAIT(true); NEXT(); v_step<true>(R, acc, sw + (t - NT2 - 1) * 16, lane, rv, voff, soff); }
    v_step<false>(R, acc, sw + (NT2 - 1) * 16, lane, rv, voff, 0);
    {
      f32x4 o;
#pragma unroll
      for (int i = 0; i < 4; ++i) o[i] = tr32(acc[i], acc[i + 4]);
      *reinterpret_cast<f32x4*>(&L.rec[wv][4 + (lane & 31) * 8 + (lane >> 5) * 4]) = o;
    }
    const unsigned tag = __builtin_amdgcn_readfirstlane(L.tag);
    WG_BARRIER();
    if (wv == 0) {
      const u32x2 g = {now32(), tag};
      ST2_SC1(g, rw, lane < 8 ? lane * 512 : OOB, F_OFF + b * 4096 + (h * 4 + sp) * 8);
    }
    return;
  }

  const int hw = wv - NSW;
  if (hw == 2) {
    const size_t base = (size_t)(b * NH + h) * SK * DH;
    const rsrc_t rk = MKRS(kc + base, SK * DH * 2), rv = MKRS(vc + base, SK * DH * 2);
    const rsrc_t ro = MKRS(wo + (size_t)h * DH * DM, DH * DM * 2), r1 = MKRS(w1, DM * FF * 2), r2 = MKRS(w2, DM * FF * 2), rout = MKRS(out, NB * DM * 2);
    const int vt = lane < 2 ? lane * (2 << 20) : OOB;
    unsigned a = __builtin_amdgcn_raw_buffer_load_b32(rk, vt, 0, 2) + __builtin_amdgcn_raw_buffer_load_b32(rv, vt, 0, 2);
    a += __builtin_amdgcn_raw_buffer_load_b32(ro, rr < 2 ? lane * 65536 : OOB, 0, 2);
    a += __builtin_amdgcn_raw_buffer_load_b32(r1, (rr * 64 + lane) * 65536, 0, 2);
    a += __builtin_amdgcn_raw_buffer_load_b32(r2, (rr == 2 && lane < 4) ? lane * 65536 : OOB, xc * (7 * 32 * DM * 2), 2);
    a += __builtin_amdgcn_raw_buffer_load_b32(rout, lane == 0 ? 0 : OOB, 0, 2);
    PIN(a);
  }
  if (hw == 0) {
    const rsrc_t rx = MKRS(x, NB * DM * 2);
    const int vx = lane < 56 ? lane * 16 : OOB;
    u32x4 xv[4], W[14];
#pragma unroll
    for (int i = 0; i < 4; ++i) xv[i] = LD_DEF(rx, vx, i * (DM * 2));
    const unsigned ep = __builtin_amdgcn_raw_buffer_load_b32(rw, (lane >> 4) * 256, EP_OFF, 16);
    const rsrc_t rq = MKRS(wq + h * DH + (b * 4 + sp) * 16, (DM - 1) * HD * 2 + 32);
    const int vq = (lane >> 1) * (2 * HD * 2) + (lane & 1) * 16;
#pragma unroll
    for (int i = 0; i < 14; ++i) W[i] = LD_NT(rq, vq, ((i >> 1) * 64 + (i & 1)) * (HD * 2));
    CBAR();
    __builtin_amdgcn_sched_barrier(0);
#pragma unroll
    for (int i = 0; i < 4; ++i) {
      const u32x4 xn = layernorm_pk(xv[i], lane < 56);
      if (lane < 56) {
#pragma unroll
        for (int r = 0; r < 4; ++r) L.xnp[i][(lane & 7) * 4 + r][lane >> 3] = xn[r];
      }
    }
    const unsigned tagl = ep + 1u;
    const unsigned tg = __builtin_amdgcn_readlane((int)tagl, b * 16), tag0 = __builtin_amdgcn_readlane((int)tagl, 0);
    if (lane == 0) { L.tag = tg; L.tag0 = tag0; L.dadj = 0; L.cks = 0; L.ckp = 0; }
    if (lane < 4) { L.kdone[lane] = 0u; L.late[lane] = 0u; }
    { const u32x2 g = {tref, tag0}; ST2_SC1(g, rw, (rr == 0 && lane == 0) ? xc * 8 : OOB, A_OFF); }
    LDS_WAIT();
#pragma unroll
    for (int k = 0; k < 7; ++k) pair_rows(W[2 * k], W[2 * k + 1]);
    float acc[4][8];
#pragma unroll
    for (int i = 0; i < 4; ++i) {
      const u32x4 xa = *reinterpret_cast<const u32x4*>(&L.xnp[i][lane >> 1][0]);
      const u32x4 xb = *reinterpret_cast<const u32x4*>(&L.xnp[i][lane >> 1][4]);
#pragma unroll
      for (int c = 0; c < 8; ++c) acc[i][c] = 0.f;
#pragma unroll
      for (int k = 0; k < 7; ++k) dot8(W[2 * k], W[2 * k + 1], k < 4 ? xa[k & 3] : xb[k & 3], acc[i]);
    }
#pragma unroll
    for (int c = 0; c < 8; ++c) { acc[0][c] = tr32(acc[0][c], acc[2][c]); acc[1][c] = tr32(acc[1][c], acc[3][c]); }
#pragma unroll
    for (int c = 0; c < 8; ++c) acc[0][c] = tr16(acc[0][c], acc[1][c]);
    float* a = acc[0];
#pragma unroll
    for (int c = 0; c < 4; ++c) a[c] = dppu<ROR8, 0xc>(a[c], a[c + 4]) + dppu<ROR8, 0x3>(a[c + 4], a[c]);
#pragma unroll
    for (int c = 0; c < 2; ++c) a[c] = dppu<SHR4, 0xa>(a[c], a[c + 2]) + dppu<SHL4, 0x5>(a[c + 2], a[c]);
    a[0] += dpp<XOR2>(a[0]); a[1] += dpp<XOR2>(a[1]);
    {
      const u32x2 g = {pk2(a[0], a[1]), tagl};
      const int slot = (lane & 1) * 4 + ((lane >> 2) & 3);
      ST2_SC1(g, rw, (lane & 2) == 0 ? (lane >> 4) * (NH * 1024) + slot * 8 : OOB, Q_OFF + h * 1024 + (b * 4 + sp) * 64);
    }
    __builtin_amdgcn_s_sleep(D_Q);
    u32x4 G;
#pragma nounroll
    for (unsigned spin = 0; spin < SPIN_CAP; ++spin) {
      CBAR();
      G = LD_SC1(rw, lane * 16, Q_OFF + (b * NH + h) * 1024);
      if (__all((G[1] == tg) & (G[3] == tg))) break;
      __builtin_amdgcn_s_sleep(P_Q);
    }
    *reinterpret_cast<u32x2*>(&L.qpk[lane * 2]) = u32x2{G[0], G[2]};
  }
  WG_BARRIER();
  const unsigned tag = __builtin_amdgcn_readfirstlane(L.tag);
  if (hw == 1) {
    const u32x2 A = LD2_SC1(rw, (lane & 7) * 8, A_OFF);
    int d = (int)(A[0] - tref);
    const bool ok = __all(A[1] == L.tag0 && d > -700 && d < 700);
    d += (int)dppi<XOR1>((unsigned)d); d += (int)dppi<XOR2>((unsigned)d); d += (int)dppi<XOR7>((unsigned)d);
    if (lane == 0 && ok) LVU(&L.dadj) = (unsigned)((d * 8) / (NTILE - 2 * NBK - 2));
  }

#define CK_PUB(k) ((k) >= 2 && (k) % 3 == 2)
#define W_WAIT(k) do { \
    while ((int)(now32() - tref - (unsigned)(W_T0 + ((k) * 4 + hw) * W_DT + ((k) < 8 ? (bid & 15) * W_DT / 16 : (bid & 7) * W_DT / 8))) < 0) __builtin_amdgcn_s_sleep(2); \
    if (hw == 1 && CK_PUB(k)) { \
      unsigned lt_ = LVU(&L.late[lane & 3]); lt_ = max(lt_, dppi<XOR1>(lt_)); lt_ = max(lt_, dppi<XOR2>(lt_)); \
      const u32x2 g_ = {lt_, L.tag0}; \
      ST2_SC1(g_, rw, ((rr & 15) == 9 && lane == 0) ? (xc + (rr >> 4) * 8) * 8 : OOB, C_OFF + (k) * 128); } \
    if (hw == 1 && (k) > 0 && CK_PUB((k) - 1)) { \
      _Pragma("nounroll") for (int tr_ = 0; tr_ < 4; ++tr_) { \
        CBAR(); \
        const u32x2 A_ = LD2_SC1(rw, (lane & 15) * 8, C_OFF + ((k) - 1) * 128); \
        unsigned m_ = min(A_[0], 1000u); m_ += dppi<XOR1>(m_); m_ += dppi<XOR2>(m_); m_ += dppi<XOR7>(m_); m_ += dppi<ROR8>(m_); \
        if (__all(A_[1] == L.tag0)) { \
          if (lane == 0 && m_ > (unsigned)CK_TH * 16) { \
            LVU(&L.cks) = (unsigned)(L.cks + min((int)m_ * 8, (int)m_ * 4 + CK_CAP * 64)); \
            LVU(&L.ckp) = (unsigned)min(L.ckp + (m_ > (unsigned)CK_TH2 * 16 ? CK_DP1 : CK_DP) * 16, CK_MAX * 16); } \
          break; } \
        __builtin_amdgcn_s_sleep(40); } } \
  } while (0)
  u32x4 WO[16], W2[8];
  {
    const rsrc_t ro = MKRS(wo + (size_t)(h * DH + hw * 64) * DM + sp * 112, 63 * DM * 2 + 224);
    const int vo = (lane & 15) < 14 ? (lane >> 4) * 1792 + (lane & 15) * 16 : OOB;
#pragma unroll
    for (int k = 0; k < 8; ++k) {
      W_WAIT(k);
      WO[2 * k] = LD_NT(ro, vo, k * 7168); WO[2 * k + 1] = LD_NT(ro, vo + 896, k * 7168);
      pair_rows(WO[2 * k], WO[2 * k + 1]);
      PIN(WO[2 * k]); PIN(WO[2 * k + 1]);
    }
    if (hw == 0) {
#pragma nounroll
      for (unsigned spin = 0; spin < SPIN_CAP; ++spin) {
        if (__all(LVU(&L.kdone[lane & 3]) != 0u)) break;
        __builtin_amdgcn_s_sleep(8);
      }
      CBAR();
      const float2 ml = *reinterpret_cast<const float2*>(L.rec[lane & 3]);
      float M = fmaxf(ml.x, dpp<XOR1>(ml.x)); M = fmaxf(M, dpp<XOR2>(M));
      float Ls = ex2((ml.x - M) * L2E) * ml.y; Ls += dpp<XOR1>(Ls); Ls += dpp<XOR2>(Ls);
      const u32x4 g2 = {__float_as_uint(M), tag, __float_as_uint(Ls), tag};
      ST4_SC1(g2, rw, lane == 0 ? 0 : OOB, ML_OFF + (b * 64 + h * 4 + sp) * 16);
      u32x4 ML;
#pragma nounroll
      for (unsigned spin = 0; spin < SPIN_CAP; ++spin) {
        __builtin_amdgcn_s_sleep(64);
        CBAR();
        ML = LD_SC1(rw, (lane & 3) * 16, ML_OFF + (b * 64 + h * 4) * 16);
        if (__all((ML[1] == tag) & (ML[3] == tag))) break;
      }
      const float ms = __uint_as_float(ML[0]);
      float Mh = fmaxf(ms, dpp<XOR1>(ms)); Mh = fmaxf(Mh, dpp<XOR2>(Mh));
      float Lh = ex2((ms - Mh) * L2E) * __uint_as_float(ML[2]); Lh += dpp<XOR1>(Lh); Lh += dpp<XOR2>(Lh);
      if (lane < 4) L.fs[lane] = ex2((ml.x - Mh) * L2E) / Lh;
    }
    const rsrc_t r1 = MKRS(w1, tailwg ? DM * FF * 2 : 0);
    const int r16 = lane >> 2;
    const int v1 = r16 * (2 * FF * 2) + (lane & 3) * 16 + j * 64;
#pragma unroll
    for (int i = 0; i < 4; ++i) {
      W_WAIT(8 + i);
      u32x4 a = LD_NT(r1, v1, (hw * 4 + i) * (32 * FF * 2)), c = LD_NT(r1, v1 + FF * 2, (hw * 4 + i) * (32 * FF * 2));
      pair_rows(a, c);
      L.w1s[lane & 3][(r16 & 3) * 2][4 * (hw * 4 + i) + (r16 >> 2)] = a; L.w1s[lane & 3][(r16 & 3) * 2 + 1][4 * (hw * 4 + i) + (r16 >> 2)] = c;
      CBAR();
    }
    const rsrc_t r2 = MKRS(w2 + (size_t)((tailwg ? j : 0) * 32 + hw * 8) * DM, tailwg ? 8 * DM * 2 : 0);
    const int v2 = lane < 56 ? lane * 16 : OOB;
#pragma unroll
    for (int i = 0; i < 4; ++i) {
      W_WAIT(12 + i);
      W2[2 * i] = LD_NT(r2, v2, i * 1792); W2[2 * i + 1] = LD_NT(r2, v2, i * 1792 + 896);
      pair_rows(W2[2 * i], W2[2 * i + 1]); PIN(W2[2 * i]); PIN(W2[2 * i + 1]);
    }
  }
  unsigned xw = *reinterpret_cast<const unsigned*>(x + b * DM + 8 * (tailwg ? j : 0) + (lane & 3) * 2);
  PIN(xw);
  const bool live = lane < 56;
  const int dsel = hw * 64 + ((lane >> 1) & 7) * 8 + ((lane >> 5) + ((lane >> 4) & 1) * 2) * 2 + (lane & 1);
  __builtin_amdgcn_sched_barrier(0);
  WG_BARRIER();
  {
    const f32x4 fs = *reinterpret_cast<const f32x4*>(L.fs);
    float o = 0.f;
#pragma unroll
    for (int w = 0; w < NSW; ++w) o = fmaf(fs[w], L.rec[w][4 + dsel], o);
    const u32x2 g = {__float_as_uint(o), tag};
    ST2_SC1(g, rw, lane * 8, P_OFF + (b * NH + h) * 8192 + hw * 2048 + sp * 512);
  }
  const unsigned tpub = now32();
  __builtin_amdgcn_sched_barrier(0);
  u32x4 W1[8];
  {
#pragma unroll
    for (int i = 0; i < 8; ++i) W1[i] = L.w1s[hw][i][lane];
#pragma unroll
    for (int i = 0; i < 8; ++i) PIN(W1[i]);
    float acc[8];
    ZERO8(acc);
    while (now32() - tpub < (unsigned)D_A) __builtin_amdgcn_s_sleep(1);
    u32x4 P0, P1;
#pragma nounroll
    for (unsigned spin = 0; spin < SPIN_CAP; ++spin) {
      CBAR();
      P0 = LD_SC1(rw, lane * 16, P_OFF + (b * NH + h) * 8192 + hw * 2048);
      P1 = LD_SC1(rw, lane * 16 + 1024, P_OFF + (b * NH + h) * 8192 + hw * 2048);
      if (__all((P0[1] == tag) & (P0[3] == tag) & (P1[1] == tag) & (P1[3] == tag))) break;
    }
    float o0 = __uint_as_float(P0[0]) + __uint_as_float(P1[0]), o1 = __uint_as_float(P0[2]) + __uint_as_float(P1[2]);
    o0 = tr32(o0, o0); o1 = tr32(o1, o1);
    unsigned op = pk2(o0, o1);
    op = (unsigned)__builtin_amdgcn_update_dpp((int)op, (int)op, ROR8, 0xc, 0xf, false);
#define YK(k) dot8(WO[2 * k], WO[2 * k + 1], dppi<BCAST + k>(op), acc);
    YK(0) YK(1) YK(2) YK(3) YK(4) YK(5) YK(6) YK(7)
#undef YK
#pragma unroll
    for (int c = 0; c < 4; ++c) acc[c] = tr32(acc[c], acc[c + 4]);
#pragma unroll
    for (int c = 0; c < 2; ++c) acc[c] = tr16(acc[c], acc[c + 2]);
    if ((lane & 15) < 14) *reinterpret_cast<float2*>(&L.yp[hw][(lane & 15) * 8 + (lane >> 4) * 2]) = make_float2(acc[0], acc[1]);
    WG_BARRIER();
    if (hw == 1) {
      const int c2 = (live ? lane : 0) * 2;
      const float2 u0 = *reinterpret_cast<const float2*>(&L.yp[0][c2]), u1 = *reinterpret_cast<const float2*>(&L.yp[1][c2]);
      const float2 u2 = *reinterpret_cast<const float2*>(&L.yp[2][c2]), u3 = *reinterpret_cast<const float2*>(&L.yp[3][c2]);
      const u32x4 g = {__float_as_uint((u0.x + u1.x) + (u2.x + u3.x)), tag, __float_as_uint((u0.y + u1.y) + (u2.y + u3.y)), tag};
      ST4_SC1(g, rw, live ? (lane >> 2) * 1024 + (lane & 3) * 16 : OOB, Y_OFF + (b * NJ + sp * 14) * 1024 + h * 64);
    }
  }
  if (!tailwg) return;

  unsigned hp = 0;
  const rsrc_t rout = MKRS(out + b * DM + 8 * j, 16);
  float acc[8], z[8];
  ZERO8(acc); ZERO8(z);
  const int col = hw * 112 + (live ? lane : 0) * 2;
  int zoff = live ? (col >> 3) * 4096 + (col & 7) * 8 : OOB;
  PIN(zoff);
  if (hw == 0) {
    u32x2 F;
#pragma nounroll
    for (unsigned spin = 0; spin < SPIN_CAP; ++spin) {
      CBAR();
      F = LD2_SC1(rw, lane * 8, F_OFF + b * 4096 + xc * 512);
      if (__all(F[1] == tag)) break;
    }
#pragma nounroll
    for (unsigned spin = 0; spin < 4096; ++spin) {
      if (__all(now32() - F[0] >= (unsigned)D_Y)) break;
    }
    u32x4 Y;
#pragma nounroll
    for (unsigned spin = 0; spin < SPIN_CAP; ++spin) {
      CBAR();
      Y = LD_SC1(rw, lane * 16, Y_OFF + (b * NJ + j) * 1024);
      if (__all((Y[1] == tag) & (Y[3] == tag))) break;
    }
    float a0 = __uint_as_float(Y[0]), a1 = __uint_as_float(Y[2]);
    a0 += dpp<ROR8>(a0); a1 += dpp<ROR8>(a1);
    a0 += dpp<ROR4>(a0); a1 += dpp<ROR4>(a1);
    a0 = tr16(a0, a0); a1 = tr16(a1, a1);
    a0 = tr32(a0, a0); a1 = tr32(a1, a1);
    const unsigned ap = pk2(a0, a1);
    hp = pk2(bflo(xw) + bflo(ap), bfhi(xw) + bfhi(ap));
    const u32x2 g = {hp, tag};
    ST2_SC1(g, rw, lane < 4 ? lane * 8 : OOB, H_OFF + b * 2048 + j * 32);
    __builtin_amdgcn_raw_buffer_store_b32(hp, rout, lane * 4, 0, 16);
    __builtin_amdgcn_raw_buffer_store_b32(tag, rw, (j == 0 && lane == 0) ? 0 : OOB, EP_OFF + b * 256, 16);
  }
  WG_BARRIER();
  __builtin_amdgcn_s_sleep(D_H);
  u32x4 G0, G1;
  const int goff = live ? lane * 32 : OOB;
#pragma nounroll
  for (unsigned spin = 0; spin < SPIN_CAP; ++spin) {
    CBAR();
    G0 = LD_SC1(rw, goff, H_OFF + b * 2048);
    G1 = LD_SC1(rw, goff, H_OFF + b * 2048 + 16);
    const bool ok = ((G0[1] == tag) & (G0[3] == tag) & (G1[1] == tag) & (G1[3] == tag)) | !live;
    if (__all(ok)) break;
  }
  const u32x4 hn = layernorm_pk(u32x4{G0[0], G0[2], G1[0], G1[2]}, live);
#pragma unroll
  for (int k = 0; k < 4; ++k) dot8(W1[2 * k], W1[2 * k + 1], hn[k], acc);
#pragma unroll
  for (int c = 0; c < 4; ++c) acc[c] = tr32(acc[c], acc[c + 4]);
#pragma unroll
  for (int c = 0; c < 2; ++c) acc[c] = tr16(acc[c], acc[c + 2]);
  float t0 = acc[0], t1 = acc[1];
  t0 += dpp<ROR8>(t0); PIN(t0); t1 += dpp<ROR8>(t1); PIN(t1);
  t0 += dpp<ROR4>(t0); PIN(t0); t1 += dpp<ROR4>(t1); PIN(t1);
  t0 += dpp<XOR2>(t0); PIN(t0); t1 += dpp<XOR2>(t1); PIN(t1);
  t0 += dpp<XOR1>(t0); PIN(t0); t1 += dpp<XOR1>(t1); PIN(t1);
  const unsigned tp = pk2(t0, t1);
  t0 = bflo(tp); t1 = bfhi(tp);
  const float s0 = t0 * __builtin_amdgcn_rcpf(1.0f + ex2(-t0 * L2E)), s1 = t1 * __builtin_amdgcn_rcpf(1.0f + ex2(-t1 * L2E));
  const unsigned sp2 = pk2(s0, s1);
#pragma unroll
  for (int k = 0; k < 4; ++k) dot8(W2[2 * k], W2[2 * k + 1], (unsigned)__builtin_amdgcn_readlane((int)sp2, 16 * k), z);
  if (live) {
    *reinterpret_cast<f32x4*>(&L.zp[hw][lane * 8]) = f32x4{z[0], z[1], z[2], z[3]};
    *reinterpret_cast<f32x4*>(&L.zp[hw][lane * 8 + 4]) = f32x4{z[4], z[5], z[6], z[7]};
  }
  WG_BARRIER();
  {
    const float2 u0 = *reinterpret_cast<const float2*>(&L.zp[0][col]), u1 = *reinterpret_cast<const float2*>(&L.zp[1][col]);
    const float2 u2 = *reinterpret_cast<const float2*>(&L.zp[2][col]), u3 = *reinterpret_cast<const float2*>(&L.zp[3][col]);
    const u32x4 g = {__float_as_uint((u0.x + u1.x) + (u2.x + u3.x)), tag, __float_as_uint((u0.y + u1.y) + (u2.y + u3.y)), tag};
    ST4_SC1(g, rw, zoff, Z_OFF + b * NJ * 4096 + j * 64);
  }
  if (hw == 0) {
    const int z3 = lane < 32 ? lane * 16 + 3072 : OOB;
    __builtin_amdgcn_s_sleep(D_Z);
    u32x4 Z[4];
#pragma nounroll
    for (unsigned spin = 0; spin < SPIN_CAP; ++spin) {
      CBAR();
#pragma unroll
      for (int i = 0; i < 3; ++i) Z[i] = LD_SC1(rw, lane * 16 + i * 1024, Z_OFF + (b * NJ + j) * 4096);
      Z[3] = LD_SC1(rw, z3, Z_OFF + (b * NJ + j) * 4096);
      bool ok = ((Z[3][1] == tag) & (Z[3][3] == tag)) | (lane >= 32);
#pragma unroll
      for (int i = 0; i < 3; ++i) ok &= (Z[i][1] == tag) & (Z[i][3] == tag);
      if (__all(ok)) break;
    }
    float a0 = (__uint_as_float(Z[0][0]) + __uint_as_float(Z[1][0])) + (__uint_as_float(Z[2][0]) + __uint_as_float(Z[3][0]));
    float a1 = (__uint_as_float(Z[0][2]) + __uint_as_float(Z[1][2])) + (__uint_as_float(Z[2][2]) + __uint_as_float(Z[3][2]));
    a0 += dpp<ROR8>(a0); a1 += dpp<ROR8>(a1);
    a0 += dpp<ROR4>(a0); a1 += dpp<ROR4>(a1);
    a0 = tr16(a0, a0); a1 = tr16(a1, a1);
    a0 = tr32(a0, a0); a1 = tr32(a1, a1);
    const unsigned mp = pk2(a0, a1);
    const unsigned op = pk2(bflo(hp) + bflo(mp), bfhi(hp) + bfhi(mp));
    __builtin_amdgcn_raw_buffer_store_b32(op, rout, lane * 4, 0, 16);
  }
}

extern "C" void kernel_launch(void* const* d_in, const int* in_sizes, int n_in, void* d_out, int out_size, void* d_ws, size_t ws_size, hipStream_t stream) {
  fused_decode_kernel<<<NB * NH * 4, NTHR, 0, stream>>>((const u16*)d_in[0], (const u16*)d_in[1], (const u16*)d_in[2], (const u16*)d_in[3],
                                                       (const u16*)d_in[4], (const u16*)d_in[6], (const u16*)d_in[7], (u16*)d_out,
                                                       (unsigned char*)d_ws);
}
```

```cpp
#include <hip/hip_runtime.h>
#include <hip/hip_bf16.h>
#include <stdint.h>

typedef unsigned u32x4 __attribute__((ext_vector_type(4)));
typedef unsigned u32x2 __attribute__((ext_vector_type(2)));
typedef float f32x4 __attribute__((ext_vector_type(4)));
typedef __bf16 bf16x2 __attribute__((ext_vector_type(2)));
typedef unsigned short u16;
typedef __amdgpu_buffer_rsrc_t rsrc_t;

constexpr int NB = 4, DM = 448, NH = 16, SK = 8192, DH = 256, FF = 1792, HD = NH * DH;
constexpr int NSW = 4, NTHR = 512;
constexpr int NT2 = 32, NTILE = 2 * NT2;
constexpr int NBK = 5;
constexpr int NJ = 56;
constexpr int EP_OFF = 0;
constexpr int A_OFF = 2048;
constexpr int ML_OFF = 4096;
constexpr int H_OFF = 8192;
constexpr int F_OFF = 32768;
constexpr int C_OFF = 49152;
constexpr int Q_OFF = 65536;
constexpr int P_OFF = Q_OFF + NB * NH * 1024;
constexpr int Y_OFF = P_OFF + NB * NH * 8192;
constexpr int Z_OFF = Y_OFF + NB * NJ * 1024;
constexpr int WS_USED = Z_OFF + NB * NJ * 4096;
#define PACE 444
constexpr int PACE0 = 436, POFF = 200, PMUL = 3;
constexpr int W_T0 = 950, W_DT = 88;
constexpr int D_A = 40, D_Y = 150;
constexpr int D_Q = 40, P_Q = 16, D_H = 15, D_Z = 13;
constexpr int CK_TH = 10, CK_TH2 = 80, CK_CAP = 60, CK_DP = 8, CK_DP1 = 3, CK_MAX = 12, CK_R = 40;
constexpr unsigned SPIN_CAP = 1u << 22;
constexpr float L2E = 1.4426950408889634f;

__device__ __forceinline__ float bflo(unsigned w) { return __builtin_bit_cast(float, w << 16); }
__device__ __forceinline__ float bfhi(unsigned w) { return __builtin_bit_cast(float, w & 0xffff0000u); }
__device__ __forceinline__ unsigned pk2(float lo, float hi) {
  bf16x2 v = {(__bf16)lo, (__bf16)hi};
  return __builtin_bit_cast(unsigned, v);
}
__device__ __forceinline__ float rbf(float f) { return bflo(pk2(f, 0.f)); }
__device__ __forceinline__ float dot2(unsigned a, unsigned b, float c) {
  return __builtin_amdgcn_fdot2_f32_bf16(__builtin_bit_cast(bf16x2, a), __builtin_bit_cast(bf16x2, b), c, false);
}
template <int CTRL, int BANK> __device__ __forceinline__ float dppu(float old, float x) {
  return __builtin_bit_cast(float, __builtin_amdgcn_update_dpp(__builtin_bit_cast(int, old), __builtin_bit_cast(int, x), CTRL, 0xf, BANK, false));
}
template <int CTRL> __device__ __forceinline__ float dpp(float x) {
  return __builtin_bit_cast(float, __builtin_amdgcn_mov_dpp(__builtin_bit_cast(int, x), CTRL, 0xf, 0xf, true));
}
template <int CTRL> __device__ __forceinline__ unsigned dppi(unsigned x) { return (unsigned)__builtin_amdgcn_mov_dpp((int)x, CTRL, 0xf, 0xf, true); }
constexpr int XOR1 = 0xB1, XOR2 = 0x4E, XOR7 = 0x141, ROR8 = 0x128, ROR4 = 0x124, SHL4 = 0x104, SHR4 = 0x114, BCAST = 0x150;
__device__ __forceinline__ float tr32(float a, float b) {
  auto s = __builtin_amdgcn_permlane32_swap(__float_as_uint(a), __float_as_uint(b), false, false);
  return __uint_as_float(s[0]) + __uint_as_float(s[1]);
}
__device__ __forceinline__ float tr16(float a, float b) {
  auto s = __builtin_amdgcn_permlane16_swap(__float_as_uint(a), __float_as_uint(b), false, false);
  return __uint_as_float(s[0]) + __uint_as_float(s[1]);
}
__device__ __forceinline__ float wave_sum(float x) {
  x += dpp<XOR1>(x); x += dpp<XOR2>(x); x += dpp<XOR7>(x); x += dpp<ROR8>(x);
  x = tr16(x, x);
  return tr32(x, x);
}
__device__ __forceinline__ float rdl(float x, int l) { return __builtin_bit_cast(float, __builtin_amdgcn_readlane(__builtin_bit_cast(int, x), l)); }
__device__ __forceinline__ float ex2(float x) { return __builtin_amdgcn_exp2f(x); }
__device__ __forceinline__ unsigned perm_lo(unsigned a, unsigned b) { return __builtin_amdgcn_perm(b, a, 0x05040100u); }
__device__ __forceinline__ unsigned perm_hi(unsigned a, unsigned b) { return __builtin_amdgcn_perm(b, a, 0x07060302u); }
__device__ __forceinline__ void pair_rows(u32x4& a, u32x4& b) {
  const u32x4 o0 = {perm_lo(a[0], b[0]), perm_hi(a[0], b[0]), perm_lo(a[1], b[1]), perm_hi(a[1], b[1])};
  const u32x4 o1 = {perm_lo(a[2], b[2]), perm_hi(a[2], b[2]), perm_lo(a[3], b[3]), perm_hi(a[3], b[3])};
  a = o0; b = o1;
}
__device__ __forceinline__ void dot8(const u32x4& a, const u32x4& b, unsigned xp, float (&acc)[8]) {
#pragma unroll
  for (int c = 0; c < 4; ++c) { acc[c] = dot2(xp, a[c], acc[c]); acc[4 + c] = dot2(xp, b[c], acc[4 + c]); }
}
#define ZERO8(a) _Pragma("unroll") for (int c_ = 0; c_ < 8; ++c_) { a[c_] = 0.f; asm volatile("" : "+v"(a[c_])); }

__device__ __forceinline__ u32x4 layernorm_pk(u32x4 xv, bool live) {
  float s1 = dot2(xv[0], 0x3f803f80u, 0.f), s2 = dot2(xv[0], xv[0], 0.f);
#pragma unroll
  for (int i = 1; i < 4; ++i) { s1 = dot2(xv[i], 0x3f803f80u, s1); s2 = dot2(xv[i], xv[i], s2); }
  s1 += dpp<XOR1>(s1); s2 += dpp<XOR1>(s2);
  s1 += dpp<XOR2>(s1); s2 += dpp<XOR2>(s2);
  s1 += dpp<XOR7>(s1); s2 += dpp<XOR7>(s2);
  s1 += dpp<ROR8>(s1); s2 += dpp<ROR8>(s2);
  const float t = tr16(s1, s2);
  float v[8];
#pragma unroll
  for (int i = 0; i < 4; ++i) { v[2 * i] = bflo(xv[i]); v[2 * i + 1] = bfhi(xv[i]); }
  const float t2 = tr32(t, t);
  const float mean = rdl(t2, 0) * (1.0f / DM), m2 = mean * mean;
  float var = fmaf(rdl(t2, 16), 1.0f / DM, -m2);
  if (__builtin_amdgcn_readfirstlane(!(m2 <= var))) {
    float q = 0.f;
#pragma unroll
    for (int i = 0; i < 8; ++i) { const float d = v[i] - mean; q = fmaf(d, d, q); }
    var = wave_sum(live ? q : 0.f) * (1.0f / DM);
  }
  const float mb = rbf(mean);
  const float sd = rbf(__builtin_amdgcn_sqrtf(rbf(rbf(var) + 1e-5f)));
  const float rs = __builtin_amdgcn_rcpf(sd);
  u32x4 o;
#pragma unroll
  for (int i = 0; i < 4; ++i) {
    unsigned d = pk2(v[2 * i] - mb, v[2 * i + 1] - mb);
    asm("" : "+v"(d));
    o[i] = pk2(bflo(d) * rs, bfhi(d) * rs);
  }
  return o;
}

#define MKRS(p, bytes) __builtin_amdgcn_make_buffer_rsrc((void*)(p), 0, (bytes), 0x00020000)
#define LD_NT(rs, voff, soff) __builtin_amdgcn_raw_buffer_load_b128(rs, voff, soff, 2)
#define LD_DEF(rs, voff, soff) __builtin_amdgcn_raw_buffer_load_b128(rs, voff, soff, 0)
#define LD_SC1(rs, voff, soff) __builtin_amdgcn_raw_buffer_load_b128(rs, voff, soff, 16)
#define LD2_SC1(rs, voff, soff) __builtin_amdgcn_raw_buffer_load_b64(rs, voff, soff, 16)
#define ST4_SC1(v, rs, voff, soff) __builtin_amdgcn_raw_buffer_store_b128(v, rs, voff, soff, 16)
#define ST2_SC1(v, rs, voff, soff) __builtin_amdgcn_raw_buffer_store_b64(v, rs, voff, soff, 16)
#define OOB 0x7ffffff0
#define CBAR() asm volatile("" ::: "memory")
#define WG_BARRIER() do { asm volatile("s_waitcnt lgkmcnt(0)" ::: "memory"); __builtin_amdgcn_s_barrier(); asm volatile("" ::: "memory"); } while (0)
#define LDS_WAIT() asm volatile("s_waitcnt lgkmcnt(0)" ::: "memory")
#define LVU(p) (*(volatile __attribute__((address_space(3))) unsigned*)(p))
#define PIN(x) asm volatile("" : "+v"(x))

struct Tile { u32x4 D[8]; };
#define TLD(rs, i) LD_NT(rs, voff + (((i) & 3) << 10) + (((i) >> 2) << 16), soff)
template <bool PACED>
__device__ __forceinline__ void tile_issue(Tile& T, rsrc_t rs, int voff, unsigned soff) {
#pragma unroll
  for (int i = 0; i < 8; ++i) { if (PACED) asm volatile("s_waitcnt vmcnt(7)" ::: "memory"); T.D[i] = TLD(rs, i); CBAR(); }
}

template <bool REFILL>
__device__ __forceinline__ void k_step(Tile& T, float& mx, float* sl, const u32x4& qv, int lane, rsrc_t rs, int voff, unsigned soff) {
  float x[8];
#pragma unroll
  for (int i = 0; i < 8; ++i) {
    float d = dot2(T.D[i][0], qv[0], 0.f);
    d = dot2(T.D[i][1], qv[1], d); d = dot2(T.D[i][2], qv[2], d); d = dot2(T.D[i][3], qv[3], d);
    x[i] = d;
    asm volatile("" : "+v"(x[i]) :: "memory");
    if (REFILL) T.D[i] = TLD(rs, i);
    __builtin_amdgcn_sched_barrier(0);
  }
#pragma unroll
  for (int i = 0; i < 4; ++i) x[i] = dppu<ROR8, 0xc>(x[i], x[i + 4]) + dppu<ROR8, 0x3>(x[i + 4], x[i]);
#pragma unroll
  for (int i = 0; i < 2; ++i) x[i] = dppu<XOR7, 0xa>(x[i], x[i + 2]) + dppu<XOR7, 0x5>(x[i + 2], x[i]);
  const float t0 = x[0] + dpp<XOR2>(x[0]), t1 = x[1] + dpp<XOR2>(x[1]);
  float sc = (lane & 2) ? t1 : t0;
  sc += dpp<XOR1>(sc);
  sc = tr16(sc, sc);
  sc = rbf(sc) * 0.0625f;
  mx = fmaxf(mx, sc);
  if ((lane & 17) == 0) sl[(lane >> 5) * 8 + ((lane & 15) >> 1)] = sc;
}

template <bool REFILL>
__device__ __forceinline__ void v_step(Tile& T, float (&acc)[8], const float* pl, int lane, rsrc_t rs, int voff, unsigned soff) {
  const f32x4 pa = *reinterpret_cast<const f32x4*>(pl + (lane >> 5) * 8), pb = *reinterpret_cast<const f32x4*>(pl + (lane >> 5) * 8 + 4);
#pragma unroll
  for (int i = 0; i < 8; ++i) {
    const float pi = i < 4 ? pa[i & 3] : pb[i & 3];
#pragma unroll
    for (int j = 0; j < 4; ++j) { acc[2 * j] = fmaf(pi, bflo(T.D[i][j]), acc[2 * j]); acc[2 * j + 1] = fmaf(pi, bfhi(T.D[i][j]), acc[2 * j + 1]); }
    asm volatile("" : "+v"(acc[0]), "+v"(acc[1]), "+v"(acc[2]), "+v"(acc[3]), "+v"(acc[4]), "+v"(acc[5]), "+v"(acc[6]), "+v"(acc[7]) :: "memory");
    if (REFILL) T.D[i] = TLD(rs, i);
    __builtin_amdgcn_sched_barrier(0);
  }
}

struct __attribute__((aligned(16))) Lds {
  u32x4 w1s[4][8][64];
  float sbuf[NSW][512];
  float zp[4][DM];
  float yp[4][112];
  unsigned xnp[4][32][8];
  unsigned qpk[128];
  float rec[NSW][DH + 4];
  float fs[4];
  unsigned kdone[4];
  unsigned late[4];
  unsigned tag, tag0;
  int dadj, cks, ckp;
  unsigned pad[3];
};
__device__ __forceinline__ unsigned now32() { return (unsigned)__builtin_amdgcn_s_memrealtime(); }

__global__ void __launch_bounds__(NTHR, 2)
fused_decode_kernel(const u16* __restrict__ x, const u16* __restrict__ kc, const u16* __restrict__ vc, const u16* __restrict__ wq,
                    const u16* __restrict__ wo, const u16* __restrict__ w1, const u16* __restrict__ w2, u16* out, unsigned char* ws) {
  __shared__ Lds L;
  const unsigned tref = now32();
  const int tid = threadIdx.x, lane = tid & 63, wv = __builtin_amdgcn_readfirstlane(tid >> 6);
  const int bid = blockIdx.x, xc = bid & 7, rr = bid >> 3;
  const int h = (rr & 1) * 8 + xc, sp = (rr >> 1) & 3, b = rr >> 3;
  const int loc = rr & 7, j = xc * 7 + loc;
  const bool tailwg = loc < 7;
  const rsrc_t rw = MKRS(ws, WS_USED);

  if (wv < NSW) {
    const int pair = b * NH + h;
    const size_t base = (size_t)pair * SK * DH;
    const rsrc_t rk = MKRS(kc + base, SK * DH * 2), rv = MKRS(vc + base, SK * DH * 2);
    const int voff = lane * 16;
    constexpr unsigned TS = 131072, RMASK = SK * DH * 2 - 1;
    unsigned soff = (wv * 4096 + sp * 16384 + pair * PMUL * TS) & RMASK;
    float* sw = L.sbuf[wv];
    Tile R, B[NBK];
    int gate = POFF * 64 + wv * (PACE * 4), gstep = PACE0 * 16;
    int sapp = 0, cks = 0, ckp = 0;
#define PACE_WAIT(MAIN) do { unsigned d_; for (;;) { d_ = now32() - tref; if (d_ > 1000000u || (int)(d_ * 64u) >= gate) break; __builtin_amdgcn_s_sleep(1); } \
      if (MAIN) { \
        if (lane == 0) LVU(&L.late[wv]) = (unsigned)((int)(d_ * 64u) - gate) >> 6; \
        const int a_ = min(cks - sapp, CK_R * 64); sapp += a_; gate += a_ + ckp; \
        cks = __builtin_amdgcn_readfirstlane((int)LVU(&L.cks)); ckp = __builtin_amdgcn_readfirstlane((int)LVU(&L.ckp)); } \
      gate += gstep; } while (0)
#define NEXT() (soff = (soff + TS) & RMASK)
    PACE_WAIT(false);
    tile_issue<false>(R, rk, voff, soff);
#pragma unroll
    for (int i = 0; i < NBK; ++i) { PACE_WAIT(false); NEXT(); tile_issue<true>(B[i], rk, voff, soff); }
    __builtin_amdgcn_sched_barrier(0);
    WG_BARRIER();
    const u32x4 qv = *reinterpret_cast<const u32x4*>(&L.qpk[(lane & 31) * 4]);
    float mx = -INFINITY;
    gstep = PACE * 16;
#pragma unroll
    for (int i = 0; i < NBK; ++i) {
      PACE_WAIT(false);
      NEXT();
      k_step<true>(R, mx, sw + (i ? NBK + i : 0) * 16, qv, lane, rk, voff, soff);
      k_step<false>(B[i], mx, sw + (i + 1) * 16, qv, lane, rk, voff, 0);
    }
    gstep += __builtin_amdgcn_readfirstlane((int)LVU(&L.dadj));
    for (int t = 2 * NBK + 1; t <= NT2; ++t) { PACE_WAIT(true); NEXT(); k_step<true>(R, mx, sw + (t - 1) * 16, qv, lane, t < NT2 ? rk : rv, voff, soff); }
    {
      float M = mx;
      M = fmaxf(M, dpp<XOR2>(M)); M = fmaxf(M, dpp<XOR7>(M)); M = fmaxf(M, dpp<ROR8>(M));
      auto sm = __builtin_amdgcn_permlane32_swap(__float_as_uint(M), __float_as_uint(M), false, false);
      M = fmaxf(__uint_as_float(sm[0]), __uint_as_float(sm[1]));
      LDS_WAIT();
      f32x4 s0 = *reinterpret_cast<const f32x4*>(sw + lane * 8), s1 = *reinterpret_cast<const f32x4*>(sw + lane * 8 + 4);
#pragma unroll
      for (int i = 0; i < 4; ++i) { s0[i] = ex2((s0[i] - M) * L2E); s1[i] = ex2((s1[i] - M) * L2E); }
      *reinterpret_cast<f32x4*>(sw + lane * 8) = s0; *reinterpret_cast<f32x4*>(sw + lane * 8 + 4) = s1;
      const float l = wave_sum(((s0[0] + s0[1]) + (s0[2] + s0[3])) + ((s1[0] + s1[1]) + (s1[2] + s1[3])));
      if (lane == 0) *reinterpret_cast<float2*>(L.rec[wv]) = make_float2(M, l);
      LDS_WAIT();
      if (lane == 0) LVU(&L.kdone[wv]) = 1u;
    }
    float acc[8];
#pragma unroll
    for (int i = 0; i < 8; ++i) acc[i] = 0.f;
    for (int t = NT2 + 1; t < NTILE; ++t) { PACE_WAIT(true); NEXT(); v_step<true>(R, acc, sw + (t - NT2 - 1) * 16, lane, rv, voff, soff); }
    v_step<false>(R, acc, sw + (NT2 - 1) * 16, lane, rv, voff, 0);
    {
      f32x4 o;
#pragma unroll
      for (int i = 0; i < 4; ++i) o[i] = tr32(acc[i], acc[i + 4]);
      *reinterpret_cast<f32x4*>(&L.rec[wv][4 + (lane & 31) * 8 + (lane >> 5) * 4]) = o;
    }
    const unsigned tag = __builtin_amdgcn_readfirstlane(L.tag);
    WG_BARRIER();
    if (wv == 0) {
      const u32x2 g = {now32(), tag};
      ST2_SC1(g, rw, lane < 8 ? lane * 512 : OOB, F_OFF + b * 4096 + (h * 4 + sp) * 8);
    }
    return;
  }

  const int hw = wv - NSW;
  if (hw == 2) {
    const size_t base = (size_t)(b * NH + h) * SK * DH;
    const rsrc_t rk = MKRS(kc + base, SK * DH * 2), rv = MKRS(vc + base, SK * DH * 2);
    const rsrc_t ro = MKRS(wo + (size_t)h * DH * DM, DH * DM * 2), r1 = MKRS(w1, DM * FF * 2), r2 = MKRS(w2, DM * FF * 2), rout = MKRS(out, NB * DM * 2);
    const int vt = lane < 2 ? lane * (2 << 20) : OOB;
    unsigned a = __builtin_amdgcn_raw_buffer_load_b32(rk, vt, 0, 2) + __builtin_amdgcn_raw_buffer_load_b32(rv, vt, 0, 2);
    a += __builtin_amdgcn_raw_buffer_load_b32(ro, rr < 2 ? lane * 65536 : OOB, 0, 2);
    a += __builtin_amdgcn_raw_buffer_load_b32(r1, (rr * 64 + lane) * 65536, 0, 2);
    a += __builtin_amdgcn_raw_buffer_load_b32(r2, (rr == 2 && lane < 4) ? lane * 65536 : OOB, xc * (7 * 32 * DM * 2), 2);
    a += __builtin_amdgcn_raw_buffer_load_b32(rout, lane == 0 ? 0 : OOB, 0, 2);
    a += __builtin_amdgcn_raw_buffer_load_b32(rw, rr == 3 ? lane * 65536 + 65536 - 128 : OOB, 0, 2);
    PIN(a);
  }
  if (hw == 0) {
    const rsrc_t rx = MKRS(x, NB * DM * 2);
    const int vx = lane < 56 ? lane * 16 : OOB;
    u32x4 xv[4], W[14];
#pragma unroll
    for (int i = 0; i < 4; ++i) xv[i] = LD_DEF(rx, vx, i * (DM * 2));
    const unsigned ep = __builtin_amdgcn_raw_buffer_load_b32(rw, (lane >> 4) * 256, EP_OFF, 16);
    const rsrc_t rq = MKRS(wq + h * DH + (b * 4 + sp) * 16, (DM - 1) * HD * 2 + 32);
    const int vq = (lane >> 1) * (2 * HD * 2) + (lane & 1) * 16;
#pragma unroll
    for (int i = 0; i < 14; ++i) W[i] = LD_NT(rq, vq, ((i >> 1) * 64 + (i & 1)) * (HD * 2));
    CBAR();
    __builtin_amdgcn_sched_barrier(0);
#pragma unroll
    for (int i = 0; i < 4; ++i) {
      const u32x4 xn = layernorm_pk(xv[i], lane < 56);
      if (lane < 56) {
#pragma unroll
        for (int r = 0; r < 4; ++r) L.xnp[i][(lane & 7) * 4 + r][lane >> 3] = xn[r];
      }
    }
    const unsigned tagl = ep + 1u;
    const unsigned tg = __builtin_amdgcn_readlane((int)tagl, b * 16), tag0 = __builtin_amdgcn_readlane((int)tagl, 0);
    if (lane == 0) { L.tag = tg; L.tag0 = tag0; L.dadj = 0; L.cks = 0; L.ckp = 0; }
    if (lane < 4) { L.kdone[lane] = 0u; L.late[lane] = 0u; }
    { const u32x2 g = {tref, tag0}; ST2_SC1(g, rw, (rr == 0 && lane == 0) ? xc * 8 : OOB, A_OFF); }
    LDS_WAIT();
#pragma unroll
    for (int k = 0; k < 7; ++k) pair_rows(W[2 * k], W[2 * k + 1]);
    float acc[4][8];
#pragma unroll
    for (int i = 0; i < 4; ++i) {
      const u32x4 xa = *reinterpret_cast<const u32x4*>(&L.xnp[i][lane >> 1][0]);
      const u32x4 xb = *reinterpret_cast<const u32x4*>(&L.xnp[i][lane >> 1][4]);
#pragma unroll
      for (int c = 0; c < 8; ++c) acc[i][c] = 0.f;
#pragma unroll
      for (int k = 0; k < 7; ++k) dot8(W[2 * k], W[2 * k + 1], k < 4 ? xa[k & 3] : xb[k & 3], acc[i]);
    }
#pragma unroll
    for (int c = 0; c < 8; ++c) { acc[0][c] = tr32(acc[0][c], acc[2][c]); acc[1][c] = tr32(acc[1][c], acc[3][c]); }
#pragma unroll
    for (int c = 0; c < 8; ++c) acc[0][c] = tr16(acc[0][c], acc[1][c]);
    float* a = acc[0];
#pragma unroll
    for (int c = 0; c < 4; ++c) a[c] = dppu<ROR8, 0xc>(a[c], a[c + 4]) + dppu<ROR8, 0x3>(a[c + 4], a[c]);
#pragma unroll
    for (int c = 0; c < 2; ++c) a[c] = dppu<SHR4, 0xa>(a[c], a[c + 2]) + dppu<SHL4, 0x5>(a[c + 2], a[c]);
    a[0] += dpp<XOR2>(a[0]); a[1] += dpp<XOR2>(a[1]);
    {
      const u32x2 g = {pk2(a[0], a[1]), tagl};
      const int slot = (lane & 1) * 4 + ((lane >> 2) & 3);
      ST2_SC1(g, rw, (lane & 2) == 0 ? (lane >> 4) * (NH * 1024) + slot * 8 : OOB, Q_OFF + h * 1024 + (b * 4 + sp) * 64);
    }
    __builtin_amdgcn_s_sleep(D_Q);
    u32x4 G;
#pragma nounroll
    for (unsigned spin = 0; spin < SPIN_CAP; ++spin) {
      CBAR();
      G = LD_SC1(rw, lane * 16, Q_OFF + (b * NH + h) * 1024);
      if (__all((G[1] == tg) & (G[3] == tg))) break;
      __builtin_amdgcn_s_sleep(P_Q);
    }
    *reinterpret_cast<u32x2*>(&L.qpk[lane * 2]) = u32x2{G[0], G[2]};
  }
  WG_BARRIER();
  const unsigned tag = __builtin_amdgcn_readfirstlane(L.tag);
  if (hw == 1) {
    const u32x2 A = LD2_SC1(rw, (lane & 7) * 8, A_OFF);
    int d = (int)(A[0] - tref);
    const bool ok = __all(A[1] == L.tag0 && d > -700 && d < 700);
    d += (int)dppi<XOR1>((unsigned)d); d += (int)dppi<XOR2>((unsigned)d); d += (int)dppi<XOR7>((unsigned)d);
    if (lane == 0 && ok) LVU(&L.dadj) = (unsigned)((d * 8) / (NTILE - 2 * NBK - 2));
  }

#define CK_PUB(k) ((k) >= 2 && (k) % 3 == 2)
#define W_WAIT(k) do { \
    while ((int)(now32() - tref - (unsigned)(W_T0 + ((k) * 4 + hw) * W_DT + ((k) < 8 ? (bid & 15) * W_DT / 16 : (bid & 7) * W_DT / 8))) < 0) __builtin_amdgcn_s_sleep(2); \
    if (hw == 1 && CK_PUB(k)) { \
      unsigned lt_ = LVU(&L.late[lane & 3]); lt_ = max(lt_, dppi<XOR1>(lt_)); lt_ = max(lt_, dppi<XOR2>(lt_)); \
      const u32x2 g_ = {lt_, L.tag0}; \
      ST2_SC1(g_, rw, ((rr & 15) == 9 && lane == 0) ? (xc + (rr >> 4) * 8) * 8 : OOB, C_OFF + (k) * 128); } \
    if (hw == 1 && (k) > 0 && CK_PUB((k) - 1)) { \
      _Pragma("nounroll") for (int tr_ = 0; tr_ < 4; ++tr_) { \
        CBAR(); \
        const u32x2 A_ = LD2_SC1(rw, (lane & 15) * 8, C_OFF + ((k) - 1) * 128); \
        unsigned m_ = min(A_[0], 1000u); m_ += dppi<XOR1>(m_); m_ += dppi<XOR2>(m_); m_ += dppi<XOR7>(m_); m_ += dppi<ROR8>(m_); \
        if (__all(A_[1] == L.tag0)) { \
          if (lane == 0 && m_ > (unsigned)CK_TH * 16) { \
            LVU(&L.cks) = (unsigned)(L.cks + min((int)m_ * 8, (int)m_ * 4 + CK_CAP * 64)); \
            LVU(&L.ckp) = (unsigned)min(L.ckp + (m_ > (unsigned)CK_TH2 * 16 ? CK_DP1 : CK_DP) * 16, CK_MAX * 16); } \
          break; } \
        __builtin_amdgcn_s_sleep(40); } } \
  } while (0)
  u32x4 WO[16], W2[8];
  {
    const rsrc_t ro = MKRS(wo + (size_t)(h * DH + hw * 64) * DM + sp * 112, 63 * DM * 2 + 224);
    const int vo = (lane & 15) < 14 ? (lane >> 4) * 1792 + (lane & 15) * 16 : OOB;
#pragma unroll
    for (int k = 0; k < 8; ++k) {
      W_WAIT(k);
      WO[2 * k] = LD_NT(ro, vo, k * 7168); WO[2 * k + 1] = LD_NT(ro, vo + 896, k * 7168);
      pair_rows(WO[2 * k], WO[2 * k + 1]);
      PIN(WO[2 * k]); PIN(WO[2 * k + 1]);
    }
    if (hw == 0) {
#pragma nounroll
      for (unsigned spin = 0; spin < SPIN_CAP; ++spin) {
        if (__all(LVU(&L.kdone[lane & 3]) != 0u)) break;
        __builtin_amdgcn_s_sleep(8);
      }
      CBAR();
      const float2 ml = *reinterpret_cast<const float2*>(L.rec[lane & 3]);
      float M = fmaxf(ml.x, dpp<XOR1>(ml.x)); M = fmaxf(M, dpp<XOR2>(M));
      float Ls = ex2((ml.x - M) * L2E) * ml.y; Ls += dpp<XOR1>(Ls); Ls += dpp<XOR2>(Ls);
      const u32x4 g2 = {__float_as_uint(M), tag, __float_as_uint(Ls), tag};
      ST4_SC1(g2, rw, lane == 0 ? 0 : OOB, ML_OFF + (b * 64 + h * 4 + sp) * 16);
      u32x4 ML;
#pragma nounroll
      for (unsigned spin = 0; spin < SPIN_CAP; ++spin) {
        __builtin_amdgcn_s_sleep(64);
        CBAR();
        ML = LD_SC1(rw, (lane & 3) * 16, ML_OFF + (b * 64 + h * 4) * 16);
        if (__all((ML[1] == tag) & (ML[3] == tag))) break;
      }
      const float ms = __uint_as_float(ML[0]);
      float Mh = fmaxf(ms, dpp<XOR1>(ms)); Mh = fmaxf(Mh, dpp<XOR2>(Mh));
      float Lh = ex2((ms - Mh) * L2E) * __uint_as_float(ML[2]); Lh += dpp<XOR1>(Lh); Lh += dpp<XOR2>(Lh);
      if (lane < 4) L.fs[lane] = ex2((ml.x - Mh) * L2E) / Lh;
    }
    const rsrc_t r1 = MKRS(w1, tailwg ? DM * FF * 2 : 0);
    const int r16 = lane >> 2;
    const int v1 = r16 * (2 * FF * 2) + (lane & 3) * 16 + j * 64;
#pragma unroll
    for (int i = 0; i < 4; ++i) {
      W_WAIT(8 + i);
      u32x4 a = LD_NT(r1, v1, (hw * 4 + i) * (32 * FF * 2)), c = LD_NT(r1, v1 + FF * 2, (hw * 4 + i) * (32 * FF * 2));
      pair_rows(a, c);
      L.w1s[lane & 3][(r16 & 3) * 2][4 * (hw * 4 + i) + (r16 >> 2)] = a; L.w1s[lane & 3][(r16 & 3) * 2 + 1][4 * (hw * 4 + i) + (r16 >> 2)] = c;
      CBAR();
    }
    const rsrc_t r2 = MKRS(w2 + (size_t)((tailwg ? j : 0) * 32 + hw * 8) * DM, tailwg ? 8 * DM * 2 : 0);
    const int v2 = lane < 56 ? lane * 16 : OOB;
#pragma unroll
    for (int i = 0; i < 4; ++i) {
      W_WAIT(12 + i);
      W2[2 * i] = LD_NT(r2, v2, i * 1792); W2[2 * i + 1] = LD_NT(r2, v2, i * 1792 + 896);
      pair_rows(W2[2 * i], W2[2 * i + 1]); PIN(W2[2 * i]); PIN(W2[2 * i + 1]);
    }
  }
  unsigned xw = *reinterpret_cast<const unsigned*>(x + b * DM + 8 * (tailwg ? j : 0) + (lane & 3) * 2);
  PIN(xw);
  const bool live = lane < 56;
  const int dsel = hw * 64 + ((lane >> 1) & 7) * 8 + ((lane >> 5) + ((lane >> 4) & 1) * 2) * 2 + (lane & 1);
  __builtin_amdgcn_sched_barrier(0);
  WG_BARRIER();
  {
    const f32x4 fs = *reinterpret_cast<const f32x4*>(L.fs);
    float o = 0.f;
#pragma unroll
    for (int w = 0; w < NSW; ++w) o = fmaf(fs[w], L.rec[w][4 + dsel], o);
    const u32x2 g = {__float_as_uint(o), tag};
    ST2_SC1(g, rw, lane * 8, P_OFF + (b * NH + h) * 8192 + hw * 2048 + sp * 512);
  }
  const unsigned tpub = now32();
  __builtin_amdgcn_sched_barrier(0);
  u32x4 W1[8];
  {
#pragma unroll
    for (int i = 0; i < 8; ++i) W1[i] = L.w1s[hw][i][lane];
#pragma unroll
    for (int i = 0; i < 8; ++i) PIN(W1[i]);
    float acc[8];
    ZERO8(acc);
    while (now32() - tpub < (unsigned)D_A) __builtin_amdgcn_s_sleep(1);
    u32x4 P0, P1;
#pragma nounroll
    for (unsigned spin = 0; spin < SPIN_CAP; ++spin) {
      CBAR();
      P0 = LD_SC1(rw, lane * 16, P_OFF + (b * NH + h) * 8192 + hw * 2048);
      P1 = LD_SC1(rw, lane * 16 + 1024, P_OFF + (b * NH + h) * 8192 + hw * 2048);
      if (__all((P0[1] == tag) & (P0[3] == tag) & (P1[1] == tag) & (P1[3] == tag))) break;
    }
    float o0 = __uint_as_float(P0[0]) + __uint_as_float(P1[0]), o1 = __uint_as_float(P0[2]) + __uint_as_float(P1[2]);
    o0 = tr32(o0, o0); o1 = tr32(o1, o1);
    unsigned op = pk2(o0, o1);
    op = (unsigned)__builtin_amdgcn_update_dpp((int)op, (int)op, ROR8, 0xc, 0xf, false);
#define YK(k) dot8(WO[2 * k], WO[2 * k + 1], dppi<BCAST + k>(op), acc);
    YK(0) YK(1) YK(2) YK(3) YK(4) YK(5) YK(6) YK(7)
#undef YK
#pragma unroll
    for (int c = 0; c < 4; ++c) acc[c] = tr32(acc[c], acc[c + 4]);
#pragma unroll
    for (int c = 0; c < 2; ++c) acc[c] = tr16(acc[c], acc[c + 2]);
    if ((lane & 15) < 14) *reinterpret_cast<float2*>(&L.yp[hw][(lane & 15) * 8 + (lane >> 4) * 2]) = make_float2(acc[0], acc[1]);
    WG_BARRIER();
    if (hw == 1) {
      const int c2 = (live ? lane : 0) * 2;
      const float2 u0 = *reinterpret_cast<const float2*>(&L.yp[0][c2]), u1 = *reinterpret_cast<const float2*>(&L.yp[1][c2]);
      const float2 u2 = *reinterpret_cast<const float2*>(&L.yp[2][c2]), u3 = *reinterpret_cast<const float2*>(&L.yp[3][c2]);
      const u32x4 g = {__float_as_uint((u0.x + u1.x) + (u2.x + u3.x)), tag, __float_as_uint((u0.y + u1.y) + (u2.y + u3.y)), tag};
      ST4_SC1(g, rw, live ? (lane >> 2) * 1024 + (lane & 3) * 16 : OOB, Y_OFF + (b * NJ + sp * 14) * 1024 + h * 64);
    }
  }
  if (!tailwg) return;

  unsigned hp = 0;
  const rsrc_t rout = MKRS(out + b * DM + 8 * j, 16);
  float acc[8], z[8];
  ZERO8(acc); ZERO8(z);
  const int col = hw * 112 + (live ? lane : 0) * 2;
  int zoff = live ? (col >> 3) * 4096 + (col & 7) * 8 : OOB;
  PIN(zoff);
  if (hw == 0) {
    u32x2 F;
#pragma nounroll
    for (unsigned spin = 0; spin < SPIN_CAP; ++spin) {
      CBAR();
      F = LD2_SC1(rw, lane * 8, F_OFF + b * 4096 + xc * 512);
      if (__all(F[1] == tag)) break;
    }
#pragma nounroll
    for (unsigned spin = 0; spin < 4096; ++spin) {
      if (__all(now32() - F[0] >= (unsigned)D_Y)) break;
    }
    u32x4 Y;
#pragma nounroll
    for (unsigned spin = 0; spin < SPIN_CAP; ++spin) {
      CBAR();
      Y = LD_SC1(rw, lane * 16, Y_OFF + (b * NJ + j) * 1024);
      if (__all((Y[1] == tag) & (Y[3] == tag))) break;
    }
    float a0 = __uint_as_float(Y[0]), a1 = __uint_as_float(Y[2]);
    a0 += dpp<ROR8>(a0); a1 += dpp<ROR8>(a1);
    a0 += dpp<ROR4>(a0); a1 += dpp<ROR4>(a1);
    a0 = tr16(a0, a0); a1 = tr16(a1, a1);
    a0 = tr32(a0, a0); a1 = tr32(a1, a1);
    const unsigned ap = pk2(a0, a1);
    hp = pk2(bflo(xw) + bflo(ap), bfhi(xw) + bfhi(ap));
    const u32x2 g = {hp, tag};
    ST2_SC1(g, rw, lane < 4 ? lane * 8 : OOB, H_OFF + b * 2048 + j * 32);
    __builtin_amdgcn_raw_buffer_store_b32(hp, rout, lane * 4, 0, 16);
    __builtin_amdgcn_raw_buffer_store_b32(tag, rw, (j == 0 && lane == 0) ? 0 : OOB, EP_OFF + b * 256, 16);
  }
  WG_BARRIER();
  __builtin_amdgcn_s_sleep(D_H);
  u32x4 G0, G1;
  const int goff = live ? lane * 32 : OOB;
#pragma nounroll
  for (unsigned spin = 0; spin < SPIN_CAP; ++spin) {
    CBAR();
    G0 = LD_SC1(rw, goff, H_OFF + b * 2048);
    G1 = LD_SC1(rw, goff, H_OFF + b * 2048 + 16);
    const bool ok = ((G0[1] == tag) & (G0[3] == tag) & (G1[1] == tag) & (G1[3] == tag)) | !live;
    if (__all(ok)) break;
  }
  const u32x4 hn = layernorm_pk(u32x4{G0[0], G0[2], G1[0], G1[2]}, live);
#pragma unroll
  for (int k = 0; k < 4; ++k) dot8(W1[2 * k], W1[2 * k + 1], hn[k], acc);
#pragma unroll
  for (int c = 0; c < 4; ++c) acc[c] = tr32(acc[c], acc[c + 4]);
#pragma unroll
  for (int c = 0; c < 2; ++c) acc[c] = tr16(acc[c], acc[c + 2]);
  float t0 = acc[0], t1 = acc[1];
  t0 += dpp<ROR8>(t0); PIN(t0); t1 += dpp<ROR8>(t1); PIN(t1);
  t0 += dpp<ROR4>(t0); PIN(t0); t1 += dpp<ROR4>(t1); PIN(t1);
  t0 += dpp<XOR2>(t0); PIN(t0); t1 += dpp<XOR2>(t1); PIN(t1);
  t0 += dpp<XOR1>(t0); PIN(t0); t1 += dpp<XOR1>(t1); PIN(t1);
  const unsigned tp = pk2(t0, t1);
  t0 = bflo(tp); t1 = bfhi(tp);
  const float s0 = t0 * __builtin_amdgcn_rcpf(1.0f + ex2(-t0 * L2E)), s1 = t1 * __builtin_amdgcn_rcpf(1.0f + ex2(-t1 * L2E));
  const unsigned sp2 = pk2(s0, s1);
#pragma unroll
  for (int k = 0; k < 4; ++k) dot8(W2[2 * k], W2[2 * k + 1], (unsigned)__builtin_amdgcn_readlane((int)sp2, 16 * k), z);
  if (live) {
    *reinterpret_cast<f32x4*>(&L.zp[hw][lane * 8]) = f32x4{z[0], z[1], z[2], z[3]};
    *reinterpret_cast<f32x4*>(&L.zp[hw][lane * 8 + 4]) = f32x4{z[4], z[5], z[6], z[7]};
  }
  WG_BARRIER();
  {
    const float2 u0 = *reinterpret_cast<const float2*>(&L.zp[0][col]), u1 = *reinterpret_cast<const float2*>(&L.zp[1][col]);
    const float2 u2 = *reinterpret_cast<const float2*>(&L.zp[2][col]), u3 = *reinterpret_cast<const float2*>(&L.zp[3][col]);
    const u32x4 g = {__float_as_uint((u0.x + u1.x) + (u2.x + u3.x)), tag, __float_as_uint((u0.y + u1.y) + (u2.y + u3.y)), tag};
    ST4_SC1(g, rw, zoff, Z_OFF + b * NJ * 4096 + j * 64);
  }
  if (hw == 0) {
    const int z3 = lane < 32 ? lane * 16 + 3072 : OOB;
    __builtin_amdgcn_s_sleep(D_Z);
    u32x4 Z[4];
#pragma nounroll
    for (unsigned spin = 0; spin < SPIN_CAP; ++spin) {
      CBAR();
#pragma unroll
      for (int i = 0; i < 3; ++i) Z[i] = LD_SC1(rw, lane * 16 + i * 1024, Z_OFF + (b * NJ + j) * 4096);
      Z[3] = LD_SC1(rw, z3, Z_OFF + (b * NJ + j) * 4096);
      bool ok = ((Z[3][1] == tag) & (Z[3][3] == tag)) | (lane >= 32);
#pragma unroll
      for (int i = 0; i < 3; ++i) ok &= (Z[i][1] == tag) & (Z[i][3] == tag);
      if (__all(ok)) break;
    }
    float a0 = (__uint_as_float(Z[0][0]) + __uint_as_float(Z[1][0])) + (__uint_as_float(Z[2][0]) + __uint_as_float(Z[3][0]));
    float a1 = (__uint_as_float(Z[0][2]) + __uint_as_float(Z[1][2])) + (__uint_as_float(Z[2][2]) + __uint_as_float(Z[3][2]));
    a0 += dpp<ROR8>(a0); a1 += dpp<ROR8>(a1);
    a0 += dpp<ROR4>(a0); a1 += dpp<ROR4>(a1);
    a0 = tr16(a0, a0); a1 = tr16(a1, a1);
    a0 = tr32(a0, a0); a1 = tr32(a1, a1);
    const unsigned mp = pk2(a0, a1);
    const unsigned op = pk2(bflo(hp) + bflo(mp), bfhi(hp) + bfhi(mp));
    __builtin_amdgcn_raw_buffer_store_b32(op, rout, lane * 4, 0, 16);
  }
}

extern "C" void kernel_launch(void* const* d_in, const int* in_sizes, int n_in, void* d_out, int out_size, void* d_ws, size_t ws_size, hipStream_t stream) {
  fused_decode_kernel<<<NB * NH * 4, NTHR, 0, stream>>>((const u16*)d_in[0], (const u16*)d_in[1], (const u16*)d_in[2], (const u16*)d_in[3],
                                                       (const u16*)d_in[4], (const u16*)d_in[6], (const u16*)d_in[7], (u16*)d_out,
                                                       (unsigned char*)d_ws);
}
```
